```python
import torch

def _softmax(x, axis=-1):
    return torch.softmax(x, dim=axis)
def _gelu(x):
    return torch.nn.functional.gelu(x, approximate="tanh")
def _silu(x):
    return torch.nn.functional.silu(x)
def _sigmoid(x):
    return torch.sigmoid(x)
def _layernorm(x, axis=-1, eps=1e-5):
    m = x.mean(dim=axis, keepdim=True)
    v = x.var(dim=axis, keepdim=True, unbiased=False)
    return (x - m) / torch.sqrt(v + eps)
def _scatter_add(tgt, idx, src, axis=0):
    return tgt.clone().index_add_(axis, idx, src)
def _segment_sum(src, seg, n):
    out = torch.zeros((n,) + src.shape[1:], dtype=src.dtype)
    return out.index_add_(0, seg, src)
def _pad(x, pw):
    flat = tuple(v for pair in reversed(pw) for v in pair)
    return torch.nn.functional.pad(x, flat)

kernel_name = 'parallel_sum__ssm_conv_s4__glu_gated_geglu__prompt'

def setup_inputs():

    _w = (lambda a: (lambda t: t.to(torch.bfloat16) if t.is_floating_point() else t)(torch.as_tensor(a)))
    def _A_setup():
        B, L, D, N = 1, 2048, 448, 256
        A_re = -(lambda _x: torch.exp(torch.as_tensor(_x)))((lambda *s: torch.randn(*s, dtype=torch.bfloat16) * 6.4)(D, N))
        A_im = torch.pi * (lambda *s: torch.randn(*s, dtype=torch.bfloat16) * 0.05)(D, N)
        B_re = (lambda *s: torch.randn(*s, dtype=torch.bfloat16) * 0.1)(D, N)
        B_im = (lambda *s: torch.randn(*s, dtype=torch.bfloat16) * 0.1)(D, N)
        C_re = (lambda *s: torch.randn(*s, dtype=torch.bfloat16) * 0.1)(D, N)
        C_im = (lambda *s: torch.randn(*s, dtype=torch.bfloat16) * 0.1)(D, N)
        log_dt = (lambda *s: torch.randn(*s, dtype=torch.bfloat16) * 0.05)(D) * 0.5 - 4.0
        Dskip = (lambda *s: torch.randn(*s, dtype=torch.bfloat16) * 0.05)(D)
        return {
            "u": _w((lambda *s: torch.randn(*s, dtype=torch.bfloat16) * 0.05)(B, L, D)),
            "A_re": _w(A_re),
            "A_im": _w(A_im),
            "B_re": _w(B_re),
            "B_im": _w(B_im),
            "C_re": _w(C_re),
            "C_im": _w(C_im),
            "log_dt": _w(log_dt),
            "Dskip": _w(Dskip),
        }
    _A_in = _A_setup()

    def _B_setup():
        B, S, Dm, Df = 1, 2048, 448, 1792
        return {
            "x": _w((lambda *s: torch.randn(*s, dtype=torch.bfloat16) * 0.05)(B, S, Dm)),
            "w_gate": _w((lambda *s: torch.randn(*s, dtype=torch.bfloat16) * 0.4)(Dm, Df) * (Dm ** -0.5)),
            "w_up": _w((lambda *s: torch.randn(*s, dtype=torch.bfloat16) * 0.4)(Dm, Df) * (Dm ** -0.5)),
            "w_down": _w((lambda *s: torch.randn(*s, dtype=torch.bfloat16) * 0.4)(Df, Dm) * (Df ** -0.5)),
        }
    _B_in = _B_setup()

    return {
        "A_u": _A_in["u"],
        "A_A_re": _A_in["A_re"],
        "A_A_im": _A_in["A_im"],
        "A_B_re": _A_in["B_re"],
        "A_B_im": _A_in["B_im"],
        "A_C_re": _A_in["C_re"],
        "A_C_im": _A_in["C_im"],
        "A_log_dt": _A_in["log_dt"],
        "A_Dskip": _A_in["Dskip"],
        "B_x": _B_in["x"],
        "B_w_gate": _B_in["w_gate"],
        "B_w_up": _B_in["w_up"],
        "B_w_down": _B_in["w_down"],
    }

def reference(A_u, A_A_re, A_A_im, A_B_re, A_B_im, A_C_re, A_C_im, A_log_dt, A_Dskip, B_x, B_w_gate, B_w_up, B_w_down):
    def _A_ref(u, A_re, A_im, B_re, B_im, C_re, C_im, log_dt, Dskip):
        Bsz, L, D = u.shape
        dt = torch.exp(log_dt)[:, None]
        A = A_re + 1j * A_im
        Bc = B_re + 1j * B_im
        Cc = C_re + 1j * C_im
        logdA = dt * A
        dB = dt * Bc
        l = (lambda x, dt: x.to(dt))(torch.arange(L), A_re.dtype)
        P = torch.exp(logdA[:, :, None] * l[None, None, :])
        K = torch.einsum("dn,dnl,dn->dl", Cc, P, dB).real
        L2 = 2 * L
        Kp = _pad(K, ((0, 0), (0, L)))
        up = (lambda x, *d: x.permute(*d))(u, 0, 2, 1)
        up = _pad(up, ((0, 0), (0, 0), (0, L)))
        Kf = (lambda x, axes=(-2,-1): torch.fft.rfft2(x.float(), dim=axes))(Kp[:, None, :], axes=(-2, -1))
        Uf = (lambda x, axes=(-2,-1): torch.fft.rfft2(x.float(), dim=axes))(up[:, :, None, :], axes=(-2, -1))
        Yf = Uf * Kf[None, :, :, :]
        y = (lambda x, s, axes=(-2,-1): torch.fft.irfft2(x, s=s, dim=axes).to(torch.bfloat16))(Yf, (1, L2), axes=(-2, -1))[:, :, 0, :L]
        y = (lambda x, *d: x.permute(*d))(y, 0, 2, 1) + u * Dskip[None, None, :]
        return y

    def _B_ref(x, w_gate, w_up, w_down):
        g = torch.einsum("bsd,df->bsf", x, w_gate)
        u = torch.einsum("bsd,df->bsf", x, w_up)
        h = _gelu(g) * u
        return torch.einsum("bsf,fd->bsd", h, w_down)

    x = A_u
    xn = _layernorm(x, axis=-1)
    return x + _A_ref(xn, A_A_re, A_A_im, A_B_re, A_B_im, A_C_re, A_C_im, A_log_dt, A_Dskip) + _B_ref(xn, B_w_gate, B_w_up, B_w_down)
```

```cpp
#include <hip/hip_runtime.h>
#include <hip/hip_bf16.h>
#include <stdint.h>

typedef unsigned short u16;
using bf16x8 = __attribute__((ext_vector_type(8))) short;
using f32x4 = __attribute__((ext_vector_type(4))) float;

constexpr int L = 2048, D = 448, NS = 256, DF = 1792;

__device__ __forceinline__ float bf2f(u16 v) { return __uint_as_float(((unsigned)v) << 16); }
__device__ __forceinline__ u16 f2bf(float f) { unsigned u = __float_as_uint(f); u += 0x7fffu + ((u >> 16) & 1u); return (u16)(u >> 16); }
__device__ __forceinline__ float rbf(float f) { return bf2f(f2bf(f)); }

__global__ void k_ln(const u16* __restrict__ x, u16* __restrict__ xn, u16* __restrict__ xnT) {
  int row = blockIdx.x * 4 + (threadIdx.x >> 6), lane = threadIdx.x & 63;
  float v[7]; float s = 0;
  for (int i = 0; i < 7; ++i) { v[i] = bf2f(x[row * D + lane + 64 * i]); s += v[i]; }
  for (int m = 32; m >= 1; m >>= 1) s += __shfl_xor(s, m);
  float mean = s / D; float q = 0;
  for (int i = 0; i < 7; ++i) { float dd = v[i] - mean; q += dd * dd; }
  for (int m = 32; m >= 1; m >>= 1) q += __shfl_xor(q, m);
  float mb = rbf(mean), vb = rbf(q / D);
  float sd = rbf(sqrtf(rbf(vb + 1e-5f)));
  for (int i = 0; i < 7; ++i) { u16 o = f2bf(rbf(v[i] - mb) / sd); xn[row * D + lane + 64 * i] = o; xnT[(lane + 64 * i) * L + row] = o; }
}

__global__ void k_kgen(const u16* Are, const u16* Aim, const u16* Bre, const u16* Bim, const u16* Cre, const u16* Cim, const u16* logdt, float* Kf) {
  int d = blockIdx.x;
  __shared__ float zr[NS], zi[NS], cr[NS], ci[NS];
  float dt = rbf(expf(bf2f(logdt[d])));
  for (int n = threadIdx.x; n < NS; n += blockDim.x) {
    int i = d * NS + n;
    zr[n] = dt * bf2f(Are[i]); zi[n] = dt * bf2f(Aim[i]);
    float br = dt * bf2f(Bre[i]), bi = dt * bf2f(Bim[i]), c_r = bf2f(Cre[i]), c_i = bf2f(Cim[i]);
    cr[n] = c_r * br - c_i * bi; ci[n] = c_r * bi + c_i * br;
  }
  __syncthreads();
  for (int l = threadIdx.x; l < L; l += blockDim.x) {
    float lf = rbf((float)l); float acc = 0;
    for (int n = 0; n < NS; ++n) {
      float e = __expf(zr[n] * lf); float s, c; __sincosf(zi[n] * lf, &s, &c);
      acc += e * (cr[n] * c - ci[n] * s);
    }
    Kf[d * L + l] = acc;
  }
}

__global__ void k_conv(const float* Kf, const u16* xnT, float* yT) {
  int d = blockIdx.x;
  __shared__ float ks[L], xs[L];
  for (int i = threadIdx.x; i < L; i += blockDim.x) { ks[i] = Kf[d * L + i]; xs[i] = bf2f(xnT[d * L + i]); }
  __syncthreads();
  for (int t = threadIdx.x; t < L; t += blockDim.x) {
    float acc = 0;
    for (int s = 0; s <= t; ++s) acc += ks[t - s] * xs[s];
    yT[d * L + t] = acc;
  }
}

__device__ __forceinline__ float gelu_t(float g) {
  float u = 0.7978845608028654f * (g + 0.044715f * g * g * g);
  return 0.5f * g * (1.f + tanhf(u));
}

__global__ void k_up(const u16* xn, const u16* wg, const u16* wu, u16* h) {
  int lane = threadIdx.x & 63, w = threadIdx.x >> 6;
  int m0 = (blockIdx.y * 4 + w) * 16, n0 = blockIdx.x * 16;
  f32x4 ag = {0, 0, 0, 0}, au = {0, 0, 0, 0};
  for (int k0 = 0; k0 < D; k0 += 32) {
    bf16x8 a = *(const bf16x8*)(xn + (m0 + (lane & 15)) * D + k0 + 8 * (lane >> 4));
    bf16x8 bg, bu;
    for (int j = 0; j < 8; ++j) { int k = k0 + 8 * (lane >> 4) + j; bg[j] = wg[k * DF + n0 + (lane & 15)]; bu[j] = wu[k * DF + n0 + (lane & 15)]; }
    ag = __builtin_amdgcn_mfma_f32_16x16x32_bf16(a, bg, ag, 0, 0, 0);
    au = __builtin_amdgcn_mfma_f32_16x16x32_bf16(a, bu, au, 0, 0, 0);
  }
  for (int j = 0; j < 4; ++j) {
    int r = m0 + (lane >> 4) * 4 + j, c = n0 + (lane & 15);
    float g = rbf(ag[j]), u = rbf(au[j]);
    h[r * DF + c] = f2bf(rbf(gelu_t(g)) * u);
  }
}

__global__ void k_down(const u16* h, const u16* wd, const u16* x, const u16* xn, const float* yT, const u16* dskip, u16* out) {
  int lane = threadIdx.x & 63, w = threadIdx.x >> 6;
  int m0 = (blockIdx.y * 4 + w) * 16, n0 = blockIdx.x * 16;
  f32x4 acc = {0, 0, 0, 0};
  for (int k0 = 0; k0 < DF; k0 += 32) {
    bf16x8 a = *(const bf16x8*)(h + (m0 + (lane & 15)) * DF + k0 + 8 * (lane >> 4));
    bf16x8 b;
    for (int j = 0; j < 8; ++j) { int k = k0 + 8 * (lane >> 4) + j; b[j] = wd[k * D + n0 + (lane & 15)]; }
    acc = __builtin_amdgcn_mfma_f32_16x16x32_bf16(a, b, acc, 0, 0, 0);
  }
  for (int j = 0; j < 4; ++j) {
    int r = m0 + (lane >> 4) * 4 + j, c = n0 + (lane & 15);
    float y = rbf(yT[c * L + r]);
    float sk = rbf(bf2f(xn[r * D + c]) * bf2f(dskip[c]));
    float ya = rbf(y + sk);
    float o = rbf(bf2f(x[r * D + c]) + ya);
    out[r * D + c] = f2bf(o + rbf(acc[j]));
  }
}

extern "C" void kernel_launch(void* const* d_in, const int* in_sizes, int n_in, void* d_out, int out_size, void* d_ws, size_t ws_size, hipStream_t stream) {
  const u16* x = (const u16*)d_in[0];
  char* ws = (char*)d_ws;
  u16* xn = (u16*)ws; u16* xnT = (u16*)(ws + (2 << 20)); float* Kf = (float*)(ws + (4 << 20)); float* yT = (float*)(ws + (8 << 20)); u16* h = (u16*)(ws + (12 << 20));
  k_ln<<<L / 4, 256, 0, stream>>>(x, xn, xnT);
  k_kgen<<<D, 256, 0, stream>>>((const u16*)d_in[1], (const u16*)d_in[2], (const u16*)d_in[3], (const u16*)d_in[4], (const u16*)d_in[5], (const u16*)d_in[6], (const u16*)d_in[7], Kf);
  k_conv<<<D, 256, 0, stream>>>(Kf, xnT, yT);
  k_up<<<dim3(DF / 16, L / 64), 256, 0, stream>>>(xn, (const u16*)d_in[10], (const u16*)d_in[11], h);
  k_down<<<dim3(D / 16, L / 64), 256, 0, stream>>>(h, (const u16*)d_in[12], x, xn, yT, (const u16*)d_in[8], (u16*)d_out);
}
```

```cpp
#include <hip/hip_runtime.h>
#include <hip/hip_bf16.h>
#include <stdint.h>

typedef unsigned short u16;
typedef unsigned long long u64;
typedef short bf16x8 __attribute__((ext_vector_type(8)));
typedef short s16x4 __attribute__((ext_vector_type(4)));
typedef __bf16 bf16x2 __attribute__((ext_vector_type(2)));
typedef float f32x2 __attribute__((ext_vector_type(2)));
typedef float f32x4 __attribute__((ext_vector_type(4)));
typedef float f32x16 __attribute__((ext_vector_type(16)));
typedef unsigned u32x4 __attribute__((ext_vector_type(4)));
typedef unsigned u32x2 __attribute__((ext_vector_type(2)));
#define LAS __attribute__((address_space(3)))
#define DI __device__ __forceinline__
#define MFMA32(a, b, c) __builtin_amdgcn_mfma_f32_32x32x16_bf16((a), (b), (c), 0, 0, 0)
#define MFMA16(a, b, c) __builtin_amdgcn_mfma_f32_16x16x32_bf16((a), (b), (c), 0, 0, 0)
#define WAITV(n) asm volatile("s_waitcnt vmcnt(" #n ")" ::: "memory")
#define PIN(v) asm volatile("" : "+v"(v))
#define SB() __builtin_amdgcn_sched_barrier(0)
#define BAR() do { asm volatile("s_waitcnt lgkmcnt(0)" ::: "memory"); __builtin_amdgcn_s_barrier(); asm volatile("" ::: "memory"); } while (0)
#define RLX_AGENT __ATOMIC_RELAXED, __HIP_MEMORY_SCOPE_AGENT

constexpr int L = 2048, D = 448, NS = 256, DF = 1792;

#if defined(__HIP_DEVICE_COMPILE__)
#pragma clang attribute push(__attribute__((target("no-packed-fp32-ops"))), apply_to = function)
#endif
DI float u2f(unsigned u) { return __builtin_bit_cast(float, u); }
DI unsigned f2u(float f) { return __builtin_bit_cast(unsigned, f); }
DI bool wall(bool c) { return __builtin_amdgcn_ballot_w64(c) == __builtin_amdgcn_read_exec(); }

DI float bf2f(unsigned v) { return u2f(v << 16); }
DI unsigned pk2(float lo, float hi) { f32x2 p = {lo, hi}; return __builtin_bit_cast(unsigned, __builtin_convertvector(p, bf16x2)); }
DI float rbf(float f) { return u2f(pk2(f, 0.f) << 16); }
DI float blo(unsigned u) { return u2f(u << 16); }
DI float bhi(unsigned u) { return u2f(u & 0xffff0000u); }
DI unsigned ldsa(LAS char* l) { return (unsigned)(size_t)l; }
DI void glds16(const void* sbase, unsigned voff, unsigned ldst) {
  asm volatile("s_mov_b32 m0, %2\n\ts_nop 0\n\tglobal_load_lds_dwordx4 %0, %1" ::"v"(voff), "s"(sbase), "s"(ldst) : "memory");
}
DI void glds16_sc1(const void* sbase, unsigned voff, unsigned ldst) {
  asm volatile("s_mov_b32 m0, %2\n\ts_nop 0\n\tglobal_load_lds_dwordx4 %0, %1 sc1" ::"v"(voff), "s"(sbase), "s"(ldst) : "memory");
}
#define GLD(name, T, ins) DI void name(T& d, const void* sbase, unsigned voff) { asm volatile(ins : "=v"(d) : "v"(voff), "s"(sbase) : "memory"); }
GLD(gld_u16, unsigned, "global_load_ushort %0, %1, %2")
GLD(gld_u16_sc1, unsigned, "global_load_ushort %0, %1, %2 sc1")
GLD(gld_b32_sc1, unsigned, "global_load_dword %0, %1, %2 sc1")
GLD(gld_b64_sc1, u32x2, "global_load_dwordx2 %0, %1, %2 sc1")
GLD(gld_b128, u32x4, "global_load_dwordx4 %0, %1, %2")
GLD(gld_b128_sc1, u32x4, "global_load_dwordx4 %0, %1, %2 sc1")
DI void gst_b128_sc1(void* sbase, unsigned voff, u32x4 v) {
  asm volatile("global_store_dwordx4 %0, %1, %2 sc1\n\ts_nop 1" ::"v"(voff), "v"(v), "s"(sbase) : "memory");
}
DI bf16x8 ldsr128(LAS char* p) { return *(LAS bf16x8*)p; }
DI bf16x8 ldsr64x2(LAS char* p0, LAS char* p1) {
  s16x4 a = *(LAS s16x4*)p0, b = *(LAS s16x4*)p1;
  return __builtin_shufflevector(a, b, 0, 1, 2, 3, 4, 5, 6, 7);
}
DI bf16x8 ldstr(LAS char* p0, LAS char* p1) {
  s16x4 a = __builtin_amdgcn_ds_read_tr16_b64_v4i16((LAS s16x4*)p0);
  s16x4 b = __builtin_amdgcn_ds_read_tr16_b64_v4i16((LAS s16x4*)p1);
  return __builtin_shufflevector(a, b, 0, 1, 2, 3, 4, 5, 6, 7);
}
template <int CTRL> DI float dppf(float v) { return __builtin_bit_cast(float, __builtin_amdgcn_update_dpp(0, __builtin_bit_cast(int, v), CTRL, 0xf, 0xf, true)); }
DI float red8(float v) {
  v += dppf<0xB1>(v);
  v += dppf<0x4E>(v);
  v += dppf<0x141>(v);
  return v;
}

#define STAMP(i) do {} while (0)

struct Params {
  const u16 *x, *Are, *Aim, *Bre, *Bim, *Cre, *Cim, *logdt, *dskip, *wg, *wu, *wd;
  u16* out;
  u16* hfrag;
  u16* xnT;
  u16* yT;
  u64* stats;
  float* cs;
  u64* ctl;
};
constexpr int F_P1 = 16, F_P2 = 48, F_H = 128, F_Y = 384;
constexpr unsigned SPIN_CAP = 1u << 15;

DI void wait_flags(const u64* ctl, int idx, u64 tag) {
  for (unsigned s = 0; s < SPIN_CAP; ++s) {
    const u64 v = __hip_atomic_load(ctl + idx, RLX_AGENT);
    if (wall(v == tag)) return;
    __builtin_amdgcn_s_sleep(2);
  }
}
DI bool tag_ok(u32x2 v, u64 tag) { return wall(v[0] == (unsigned)tag && v[1] == (unsigned)(tag >> 32)); }

struct KTab { u16 v[2048]; };
constexpr KTab make_ktab() {
  KTab t{};
  for (int i = 0; i < 2048; ++i) {
    const int l = (i & ~7) + 7 - (i & 7);
    int sh = 0;
    while ((l >> sh) >= 256) ++sh;
    int q = l >> sh;
    if (sh) { const int rem = l & ((1 << sh) - 1), half = 1 << (sh - 1); if (rem > half || (rem == half && (q & 1))) ++q; }
    const int v = q << sh;
    int a = 0, b = 0;
    if (v < 256) { a = v & 7; b = v >> 3; }
    else if (v < 512) { a = 8 + ((v & 7) >> 1); b = (v & 255) >> 3; }
    else if (v < 1024) { a = 12 + ((v >> 2) & 1) + 2 * ((v >> 8) & 1); b = (v & 255) >> 3; }
    else if (v < 2048) { a = 16 + ((v >> 8) & 3); b = (v & 255) >> 3; }
    else { a = 20; b = 0; }
    t.v[i] = (u16)((a * 32 + b) * 4);
  }
  return t;
}
__device__ const KTab g_ktab = make_ktab();

struct cf { float r, i; };
DI cf cmul(cf a, cf b) { return {a.r * b.r - a.i * b.i, a.r * b.i + a.i * b.r}; }
DI cf cexp_(float zr2, float zrev, float l) {
  const float m = __builtin_amdgcn_exp2f(zr2 * l), f = __builtin_amdgcn_fractf(zrev * l);
  return {m * __builtin_amdgcn_cosf(f), m * __builtin_amdgcn_sinf(f)};
}

constexpr int P_STRIDE = 928;
DI void spare_block(const Params& p, int sp, LAS char* lds) {
  const int tid = threadIdx.x, lane = tid & 63, w = __builtin_amdgcn_readfirstlane(tid >> 6);
  const int t0 = sp * 64;
  STAMP(0);
  const u64 tag = __hip_atomic_load(p.ctl, RLX_AGENT) + 1;
  const int row = 8 * w + (lane >> 3), pp = lane & 7;
  const u32x4* src = (const u32x4*)(p.x + (size_t)(t0 + row) * D) + pp;
  u32x4 v[7];
#pragma unroll
  for (int i = 0; i < 7; ++i) v[i] = src[8 * i];
  const int js = sp < 28 ? sp : 0;
  const u32x4* wsrc = (const u32x4*)(((js & 1) ? p.wu : p.wg) + (size_t)(tid >> 4) * DF + (js >> 1) * 128) + (tid & 15);
  u32x4 wv[14];
#pragma unroll
  for (int i = 0; i < 14; ++i) wv[i] = wsrc[(size_t)i * (32 * DF / 8)];
  float s = 0.f;
#pragma unroll
  for (int i = 0; i < 7; ++i)
#pragma unroll
    for (int k = 0; k < 4; ++k) s += blo(v[i][k]) + bhi(v[i][k]);
  s = red8(s);
  const float mean = s * (1.f / D);
  float q = 0.f;
#pragma unroll
  for (int i = 0; i < 7; ++i)
#pragma unroll
    for (int k = 0; k < 4; ++k) { float a = blo(v[i][k]) - mean, b = bhi(v[i][k]) - mean; q += a * a + b * b; }
  q = red8(q);
  const float mb = rbf(mean), vb = rbf(q * (1.f / D));
  const float sd = rbf(__builtin_sqrtf(rbf(vb + 1e-5f)));
  const float rs = 1.f / sd;
  if (pp == 0) __hip_atomic_store(p.stats + t0 + row, (u64)f2u(mb) | ((u64)f2u(sd) << 32), RLX_AGENT);
  LAS char* cb = lds + 64 * P_STRIDE;
  {
    f32x4 c0 = {0, 0, 0, 0}, c1 = {0, 0, 0, 0};
#pragma unroll
    for (int i = 0; i < 14; ++i) {
      c0 += (f32x4){blo(wv[i][0]), bhi(wv[i][0]), blo(wv[i][1]), bhi(wv[i][1])};
      c1 += (f32x4){blo(wv[i][2]), bhi(wv[i][2]), blo(wv[i][3]), bhi(wv[i][3])};
    }
    *(LAS f32x4*)(cb + tid * 32) = c0;
    *(LAS f32x4*)(cb + tid * 32 + 16) = c1;
  }
#pragma unroll
  for (int i = 0; i < 7; ++i) {
    u32x4 o;
#pragma unroll
    for (int k = 0; k < 4; ++k) {
      unsigned d = pk2(blo(v[i][k]) - mb, bhi(v[i][k]) - mb);
      o[k] = pk2(blo(d) * rs, bhi(d) * rs);
    }
    *(LAS u32x4*)(lds + row * P_STRIDE + (8 * i + pp) * 16) = o;
  }
  BAR();
  if (tid < 128 && sp < 28) {
    float c = 0.f;
#pragma unroll
    for (int r = 0; r < 32; ++r) c += *(LAS float*)(cb + r * 512 + tid * 4);
    __hip_atomic_store((unsigned*)p.cs + (js & 1) * DF + (js >> 1) * 128 + tid, f2u(c), RLX_AGENT);
  }
  WAITV(0);
  BAR();
  if (tid == 0) __hip_atomic_store(p.ctl + F_P1 + sp, tag, RLX_AGENT);
  STAMP(1);
  const int g16 = lane >> 4, q4 = (lane & 15) >> 2, p4 = lane & 3;
  bf16x8 r[7];
#pragma unroll
  for (int i = 0; i < 7; ++i) {
    const int item = w * 7 + i, ct = item >> 1, rd = item & 1;
    LAS char* a = lds + (32 * rd + 8 * g16 + q4) * P_STRIDE + ct * 32 + p4 * 8;
    r[i] = ldstr(a, a + 4 * P_STRIDE);
  }
#pragma unroll
  for (int i = 0; i < 7; ++i) {
    const int item = w * 7 + i, ct = item >> 1, rd = item & 1;
    gst_b128_sc1(p.xnT, (unsigned)(((ct * 16 + (lane & 15)) * L + t0 + 32 * rd + 8 * g16) * 2), __builtin_bit_cast(u32x4, r[i]));
  }
  WAITV(0);
  BAR();
  if (tid == 0) __hip_atomic_store(p.ctl + F_P2 + sp, tag, RLX_AGENT);
  STAMP(2);
  if (sp == 0 && w == 0) {
    wait_flags(p.ctl, F_P1 + (lane & 31), tag);
#pragma unroll 1
    for (int i = 0; i < 4; ++i) { const int e = lane + 64 * i; wait_flags(p.ctl, F_H + ((e & 15) < 14 ? e : (e & ~15)), tag); }
    if (lane == 0) __hip_atomic_store(p.ctl, tag, RLX_AGENT);
  }
  STAMP(3);
}

constexpr int U_SLOT = 49408;
constexpr int K_SCR = 2 * U_SLOT, K_SW = 16 * 272, K_TAB = K_SCR + 8 * K_SW;
constexpr int C_K = 3 * U_SLOT, C_KCH = 7680;
constexpr int C_X = 114688, C_XC = 4160, C_XCH = 4 * C_XC;
constexpr int C_RED = 114688;
constexpr int D_SLOT = 49152;
constexpr int LDS_BYTES = 163840;
static_assert(C_K + 2 * C_KCH <= LDS_BYTES && C_X + 2 * C_XCH <= C_K && K_TAB + 2 * 672 * 4 <= C_K, "lds map");

DI void main_block(const Params& p, int xcd, int o, LAS char* lds) {
  const int tid = threadIdx.x, lane = tid & 63, w = __builtin_amdgcn_readfirstlane(tid >> 6);
  const int h = lane >> 5, l31 = lane & 31, blk = (lane >> 4) & 1, q4 = (lane & 15) >> 2, p4 = lane & 3;
  const int sg = ((p4 & 1) << 1) | (p4 >> 1);
  const int tt = 4 * (xcd >> 1) + o / 7, fs = 7 * (xcd & 1) + o % 7;
  const int cb = xcd * 28 + o;
  const int dtt = 4 * xcd + o / 7, ddt = o % 7;
  const int cl = w >> 2, wq = w & 3, ch = 2 * cb + cl, tc = tid & 255;
  const int tg = w >> 2, cg = w & 3;
  const int t0 = tt * 128;
  STAMP(0);
  unsigned pAr, pAi, pBr, pBi, pCr, pCi, pDt, pDk; u32x4 kidx; u32x2 ep;
  {
    const unsigned no = (unsigned)(ch * NS + tc) * 2;
    gld_b64_sc1(ep, p.ctl, 0u);
    gld_u16(pDt, p.logdt, (unsigned)ch * 2); gld_u16(pAr, p.Are, no); gld_u16(pAi, p.Aim, no); gld_u16(pBr, p.Bre, no); gld_u16(pBi, p.Bim, no);
    gld_u16(pCr, p.Cre, no); gld_u16(pCi, p.Cim, no); gld_u16(pDk, p.dskip, (unsigned)ch * 2);
    gld_b128(kidx, &g_ktab, (unsigned)tc * 16);
  }
  unsigned xo[2], wo[2];
#pragma unroll
  for (int i = 0; i < 2; ++i) {
    const int r = 8 * (2 * w + i) + (lane >> 3), c = (lane & 7) ^ ((r >> 1) & 7);
    xo[i] = (unsigned)((t0 + r) * (D * 2) + c * 16);
    const int kr = 4 * (2 * w + i) + (lane >> 4), cp = lane & 15;
    const int cc = (cp & 8) | ((((cp >> 1) & 3) ^ (kr & 3)) << 1) | (cp & 1);
    wo[i] = (unsigned)(kr * (DF * 2) + fs * 256 + cc * 16);
  }
  const unsigned lb = ldsa(lds) + w * 2048;
  auto stage_x = [&](int kc, int slot) {
    const unsigned b = lb + slot * U_SLOT;
    const char* xs = (const char*)p.x + kc * 128;
    glds16(xs, xo[0], b); glds16(xs, xo[1], b + 1024);
  };
  auto stage_g = [&](int kc, int slot) {
    const unsigned b = lb + slot * U_SLOT + 16384;
    const char* gs = (const char*)p.wg + (size_t)kc * (64 * DF * 2);
    glds16(gs, wo[0], b); glds16(gs, wo[1], b + 1024);
  };
  auto stage_u = [&](int kc, int slot) {
    const unsigned b = lb + slot * U_SLOT + 32768 + 128;
    const char* us = (const char*)p.wu + (size_t)kc * (64 * DF * 2);
    glds16(us, wo[0], b); glds16(us, wo[1], b + 1024);
  };
#pragma unroll
  for (int c = 0; c < 2; ++c) { stage_x(c, c); stage_g(c, c); stage_u(c, c); }
  if (tc < 160) *(LAS u32x4*)(lds + C_K + cl * C_KCH + tc * 16) = (u32x4){0, 0, 0, 0};
  WAITV(12);
  PIN(ep); PIN(pDt); PIN(pAr); PIN(pAi); PIN(pBr); PIN(pBi); PIN(pCr); PIN(pCi); PIN(pDk); PIN(kidx);
  const u64 tag = (((u64)__builtin_amdgcn_readfirstlane(ep[1]) << 32) | (unsigned)__builtin_amdgcn_readfirstlane(ep[0])) + 1;
  STAMP(1);
  {
    const float dt = rbf(__builtin_amdgcn_exp2f(bf2f(pDt) * 1.4426950408889634f));
    const float zr2 = dt * bf2f(pAr) * 1.4426950408889634f, zrev = dt * bf2f(pAi) * 0.15915494309189535f;
    const cf cc = cmul({bf2f(pCr), bf2f(pCi)}, {dt * bf2f(pBr), dt * bf2f(pBi)});
    LAS char* ew = lds + K_SCR + w * K_SW;
    LAS char* ewr = ew + lane * 4;
    LAS char* erd = ew + (lane & 15) * 272 + (lane >> 4) * 16;
    bf16x8 bfr[2][4];
    {
      cf e[16];
      e[0] = {1.f, 0.f};
      e[1] = cexp_(zr2, zrev, 8.f);
      e[2] = cmul(e[1], e[1]); e[3] = cmul(e[2], e[1]);
      e[4] = cexp_(zr2, zrev, 32.f);
#pragma unroll
      for (int b = 5; b < 8; ++b) e[b] = cmul(e[4], e[b - 4]);
      e[8] = cmul(e[4], e[4]);
#pragma unroll
      for (int b = 9; b < 16; ++b) e[b] = cmul(e[8], e[b - 8]);
#pragma unroll
      for (int b = 0; b < 16; ++b) *(LAS unsigned*)(ewr + b * 272) = pk2(e[b].r, e[b].i);
#pragma unroll
      for (int s = 0; s < 4; ++s) bfr[0][s] = ldsr128(erd + s * 64);
      const cf e16 = cexp_(zr2, zrev, 128.f);
#pragma unroll
      for (int b = 0; b < 16; ++b) { const cf t = cmul(e16, e[b]); *(LAS unsigned*)(ewr + b * 272) = pk2(t.r, t.i); }
#pragma unroll
      for (int s = 0; s < 4; ++s) bfr[1][s] = ldsr128(erd + s * 64);
    }
    f32x4 kt[2][2] = {};
    {
      cf e[16];
      const cf p1 = cexp_(zr2, zrev, 1.f), p2 = cmul(p1, p1), pq = cmul(p2, p2);
      e[0] = cc; e[1] = cmul(cc, p1); e[2] = cmul(cc, p2); e[3] = cmul(e[2], p1);
      e[4] = cmul(cc, pq); e[5] = cmul(e[4], p1); e[6] = cmul(e[4], p2); e[7] = cmul(e[6], p1);
      const cf q1 = cexp_(zr2, zrev, 256.f), q2 = cexp_(zr2, zrev, 512.f), q3 = cmul(q2, q1), q4c = cexp_(zr2, zrev, 1024.f);
      e[8] = cmul(e[0], q1); e[9] = cmul(e[2], q1); e[10] = cmul(e[4], q1); e[11] = cmul(e[6], q1);
      e[12] = cmul(e[0], q2); e[13] = cmul(e[4], q2); e[14] = cmul(e[0], q3); e[15] = cmul(e[4], q3);
#pragma unroll
      for (int a = 0; a < 16; ++a) *(LAS unsigned*)(ewr + a * 272) = pk2(e[a].r, -e[a].i);
#pragma unroll
      for (int s = 0; s < 4; ++s) {
        const bf16x8 a = ldsr128(erd + s * 64);
        kt[0][0] = MFMA16(a, bfr[0][s], kt[0][0]); kt[0][1] = MFMA16(a, bfr[1][s], kt[0][1]);
      }
      cf f[5];
      f[0] = cmul(e[0], q4c); f[1] = cmul(e[8], q4c); f[2] = cmul(e[12], q4c); f[3] = cmul(e[14], q4c); f[4] = cmul(f[0], q4c);
#pragma unroll
      for (int a = 0; a < 5; ++a) *(LAS unsigned*)(ewr + a * 272) = pk2(f[a].r, -f[a].i);
#pragma unroll
      for (int s = 0; s < 4; ++s) {
        const bf16x8 a = ldsr128(erd + s * 64);
        kt[1][0] = MFMA16(a, bfr[0][s], kt[1][0]); kt[1][1] = MFMA16(a, bfr[1][s], kt[1][1]);
      }
    }
    LAS char* pw = ew + (4 * (lane >> 4)) * 128 + (lane & 15) * 4;
#pragma unroll
    for (int r = 0; r < 4; ++r) { *(LAS float*)(pw + r * 128) = kt[0][0][r]; *(LAS float*)(pw + r * 128 + 64) = kt[0][1][r]; }
    if (lane < 32) {
#pragma unroll
      for (int r = 0; r < 4; ++r) { *(LAS float*)(pw + 2048 + r * 128) = kt[1][0][r]; *(LAS float*)(pw + 2048 + r * 128 + 64) = kt[1][1][r]; }
    }
    STAMP(2);
    BAR();
    LAS char* tab = lds + K_TAB + cl * (672 * 4);
    LAS char* pb = lds + K_SCR + cl * 4 * K_SW + tc * 4;
#pragma unroll
    for (int i = 0; i < 3; ++i) {
      if (i < 2 || tc < 160) {
        float s = *(LAS float*)(pb + i * 1024) + *(LAS float*)(pb + K_SW + i * 1024);
        s += *(LAS float*)(pb + 2 * K_SW + i * 1024) + *(LAS float*)(pb + 3 * K_SW + i * 1024);
        *(LAS float*)(tab + tc * 4 + i * 1024) = s;
      }
    }
    BAR();
    u32x4 ko;
#pragma unroll
    for (int k = 0; k < 4; ++k) ko[k] = pk2(*(LAS float*)(tab + (kidx[k] & 0xffffu)), *(LAS float*)(tab + (kidx[k] >> 16)));
    *(LAS u32x4*)(lds + C_K + cl * C_KCH + (32 + (tc >> 2)) * 80 + (tc & 3) * 16) = ko;
  }
  int aoff[2], boff[4];
#pragma unroll
  for (int i = 0; i < 2; ++i) {
    const int a = 2 * cg + i;
    aoff[i] = 16384 + blk * (16384 + 128) + (8 * h + q4) * 256 + ((a >> 2) << 7) + (((a & 3) ^ q4) << 5) + sg * 8;
  }
  const int brow = 64 * tg + l31;
#pragma unroll
  for (int s = 0; s < 4; ++s) boff[s] = brow * 128 + (((2 * s + h) ^ ((brow >> 1) & 7)) << 4);
  f32x16 acc[2][2] = {};
  PIN(acc[0][0]); PIN(acc[0][1]); PIN(acc[1][0]); PIN(acc[1][1]);
  struct Frag { bf16x8 a0, a1, b0, b1; };
  auto ldfrag = [&](LAS char* sb, int s) {
    Frag f;
    f.b0 = ldsr128(sb + boff[s]);
    f.a0 = ldstr(sb + aoff[0] + s * 4096, sb + aoff[0] + s * 4096 + 1024);
    f.b1 = ldsr128(sb + boff[s] + 4096);
    f.a1 = ldstr(sb + aoff[1] + s * 4096, sb + aoff[1] + s * 4096 + 1024);
    return f;
  };
  auto mm = [&](const Frag& f) {
    acc[0][0] = MFMA32(f.a0, f.b0, acc[0][0]);
    acc[1][0] = MFMA32(f.a0, f.b1, acc[1][0]);
    acc[0][1] = MFMA32(f.a1, f.b0, acc[0][1]);
    acc[1][1] = MFMA32(f.a1, f.b1, acc[1][1]);
  };
  WAITV(6);
  BAR();
  STAMP(3);
  stage_x(2, 2); stage_g(2, 2); stage_u(2, 2);
  Frag f0 = ldfrag(lds, 0), f1;
  auto body3 = [&](LAS char* sb) {
    f1 = ldfrag(sb, 1); SB(); mm(f0); SB();
    f0 = ldfrag(sb, 2); SB(); mm(f1); SB();
    f1 = ldfrag(sb, 3); SB(); mm(f0); SB();
  };
  int slot = 0;
#pragma unroll 1
  for (int c = 0; c < 4; ++c) {
    body3(lds + slot * U_SLOT);
    const int nslot = slot == 2 ? 0 : slot + 1;
    WAITV(6);
    BAR();
    f0 = ldfrag(lds + nslot * U_SLOT, 0);
    SB();
    acc[0][0] = MFMA32(f1.a0, f1.b0, acc[0][0]); SB();
    stage_x(c + 3, slot); SB();
    acc[1][0] = MFMA32(f1.a0, f1.b1, acc[1][0]); SB();
    stage_g(c + 3, slot); SB();
    acc[0][1] = MFMA32(f1.a1, f1.b0, acc[0][1]); SB();
    stage_u(c + 3, slot); SB();
    acc[1][1] = MFMA32(f1.a1, f1.b1, acc[1][1]); SB();
    slot = nslot;
  }
  STAMP(4);
  body3(lds + 1 * U_SLOT);
  WAITV(6);
  BAR();
  f0 = ldfrag(lds + 2 * U_SLOT, 0); SB();
  const int pidx = lane >= 32 ? F_P2 + l31 : F_P1 + ((lane & 2) ? 2 * fs : 2 * tt) + (lane & 1);
  u32x2 fl; gld_b64_sc1(fl, p.ctl, (unsigned)pidx * 8); SB();
  mm(f1); SB();
  body3(lds + 2 * U_SLOT);
  WAITV(0); PIN(fl);
  BAR();
  f0 = ldfrag(lds, 0); SB();
  if (!tag_ok(fl, tag)) wait_flags(p.ctl, pidx, tag);
  u32x2 st[2]; unsigned csv[2]; u32x4 xa, xb;
  {
    gld_b64_sc1(st[0], p.stats, (unsigned)(t0 + brow) * 8); gld_b64_sc1(st[1], p.stats, (unsigned)(t0 + brow + 32) * 8);
    const int r16 = lane & 15, fo = 128 * fs + 32 * cg + 4 * (((r16 >> 2 & 1) << 1) | (r16 >> 3)) + (r16 & 3);
    gld_b32_sc1(csv[0], p.cs, (unsigned)(blk * DF + fo) * 4); gld_b32_sc1(csv[1], p.cs, (unsigned)(blk * DF + fo + 16) * 4);
    const int ia = (tc + 252) & 255;
    gld_b128_sc1(xa, p.xnT, (unsigned)(ch * L * 2 + ia * 16)); gld_b128_sc1(xb, p.xnT, (unsigned)(ch * L * 2 + ((ia + 1) & 255) * 16));
  }
  SB();
  mm(f1); SB();
  body3(lds);
  BAR();
  STAMP(5);
  const int dt2 = w & 1, g = w >> 1;
  unsigned wdo;
  { const int r8 = lane >> 3, cc = (lane & 7) ^ (((r8 >> 1) & 1) << 2); wdo = (unsigned)((448 * g + 112 * dt2 + r8) * (D * 2) + ddt * 128 + cc * 16); }
  const int wdr = g * 28672 + (8 * h + q4) * 128 + ((dt2 ^ ((q4 >> 1) & 1)) << 6) + blk * 32 + sg * 8;
  auto stage_wd = [&](int rd) {
    const char* src = (const char*)p.wd + (size_t)rd * (224 * D * 2);
    const unsigned dst = ldsa(lds) + g * 28672 + dt2 * 14336;
#pragma unroll
    for (int i = 0; i < 14; ++i) glds16(src + i * (8 * D * 2), wdo, dst + i * 1024);
  };
  acc[0][0] = MFMA32(f1.a0, f1.b0, acc[0][0]); acc[1][0] = MFMA32(f1.a0, f1.b1, acc[1][0]); SB();
  stage_wd(0); SB();
  acc[0][1] = MFMA32(f1.a1, f1.b0, acc[0][1]); acc[1][1] = MFMA32(f1.a1, f1.b1, acc[1][1]); SB();
  WAITV(14);
  PIN(st[0]); PIN(st[1]); PIN(csv[0]); PIN(csv[1]); PIN(xa); PIN(xb);
  {
    bf16x8 ca[2];
#pragma unroll
    for (int i = 0; i < 2; ++i) {
      const float c = h ? 0.f : u2f(csv[i]);
      const float c0 = rbf(c), r1 = c - c0, c1 = rbf(r1), c2 = r1 - c1;
      u32x4 t = {pk2(c0, c1), pk2(c2, 0.f), 0u, 0u};
      ca[i] = __builtin_bit_cast(bf16x8, t);
    }
#pragma unroll
    for (int j = 0; j < 2; ++j) {
      const unsigned nm = (st[j][0] >> 16) ^ 0x8000u;
      u32x4 t = {nm | (nm << 16), nm, 0u, 0u};
      bf16x8 mb = __builtin_bit_cast(bf16x8, t);
      acc[j][0] = MFMA32(ca[0], mb, acc[j][0]);
      acc[j][1] = MFMA32(ca[1], mb, acc[j][1]);
    }
    const float K1 = -2.f * 0.7978845608028654f * 1.4426950408889634f, K2 = K1 * 0.044715f;
#pragma unroll
    for (int j = 0; j < 2; ++j) {
      const float sd = u2f(st[j][1]), r = 1.f / sd, a1 = K1 * r, a2 = K2 * r * r * r, s2 = sd * sd;
#pragma unroll
      for (int i = 0; i < 2; ++i) {
        float hv[8];
#pragma unroll
        for (int e = 0; e < 8; ++e) {
          const float gg = acc[j][i][e], u = acc[j][i][e + 8];
          const float ex = __builtin_amdgcn_exp2f(gg * __builtin_fmaf(gg * gg, a2, a1));
          hv[e] = (gg * u) * __builtin_amdgcn_rcpf(__builtin_fmaf(ex, s2, s2));
        }
        u32x4 ov = {pk2(hv[0], hv[1]), pk2(hv[2], hv[3]), pk2(hv[4], hv[5]), pk2(hv[6], hv[7])};
        const int tile = 4 * tt + 2 * tg + j, fstep = 8 * fs + 2 * cg + i;
        gst_b128_sc1(p.hfrag, (unsigned)(((tile * 112 + fstep) * 64 + lane) * 16), ov);
      }
    }
  }
  STAMP(6);
  {
    LAS char* xw = lds + C_X + cl * C_XCH;
    auto build = [&](const u32x4& A, const u32x4& B, int oct) {
      const unsigned o0 = __builtin_amdgcn_alignbit(A[1], A[0], 16), o1 = __builtin_amdgcn_alignbit(A[2], A[1], 16), o2 = __builtin_amdgcn_alignbit(A[3], A[2], 16);
      const unsigned o3 = __builtin_amdgcn_alignbit(B[0], A[3], 16), o4 = __builtin_amdgcn_alignbit(B[1], B[0], 16);
      *(LAS u32x4*)(xw + 0 * C_XC + oct * 16) = (u32x4){o0, o1, o2, o3};
      *(LAS u32x4*)(xw + 1 * C_XC + oct * 16) = (u32x4){A[1], A[2], A[3], B[0]};
      *(LAS u32x4*)(xw + 2 * C_XC + oct * 16) = (u32x4){o1, o2, o3, o4};
      *(LAS u32x4*)(xw + 3 * C_XC + oct * 16) = (u32x4){A[2], A[3], B[0], B[1]};
    };
    const u32x4 z4 = {0, 0, 0, 0};
    build(xa, tc == 3 ? z4 : xb, tc >= 4 ? tc : 256 + tc);
    if (tc < 4) build(z4, tc == 3 ? xb : z4, tc);
  }
  WAITV(0);
  BAR();
  if (tid == 0) __hip_atomic_store(p.ctl + F_H + tt * 16 + fs, tag, RLX_AGENT);
  STAMP(7);
  bf16x8 afr[28];
#pragma unroll
  for (int ks = 0; ks < 14; ++ks) afr[ks] = ldstr(lds + wdr + ks * 2048, lds + wdr + ks * 2048 + 512);
  f32x16 y0 = {}, y1 = {};
  {
    LAS char* ap = lds + C_X + cl * C_XCH + (l31 & 3) * C_XC + 48 + 8 * (l31 >> 2) - 16 * h + 64 * (8 * wq);
    LAS char* ap2 = ap + 8;
    PIN(ap2);
    LAS char* bp = lds + C_K + cl * C_KCH + (32 + l31 - 8 * wq) * 80 + 16 * h;
    auto step = [&]() {
#pragma unroll
      for (int s = 0; s < 2; ++s) {
        bf16x8 a0 = ldsr64x2(ap - 32 * s, ap2 - 32 * s), a1 = ldsr64x2(ap - 32 * s + 2048, ap2 - 32 * s + 2048);
        bf16x8 b0 = ldsr128(bp + 32 * s), b1 = ldsr128(bp + 32 * s + 2560);
        y0 = MFMA32(a0, b0, y0);
        y1 = MFMA32(a1, b0, y1);
        y1 = MFMA32(a0, b1, y1);
      }
      ap += 64; ap2 += 64; bp -= 80;
    };
    step();
    BAR();
    stage_wd(1);
#pragma unroll 1
    for (int u = 1; u < 8; ++u) step();
  }
  STAMP(8);
  const int rc = wq >> 1, rg = 2 * (wq & 1) + h;
  const int yt = 32 * (32 * rc + l31) + 8 * rg;
  LAS char* xsk = lds + C_X + cl * C_XCH + 3 * C_XC + (yt + 28) * 2;
  u32x2 xs0 = *(LAS u32x2*)xsk, xs1 = *(LAS u32x2*)(xsk + 8);
  const int hidx = F_H + (dtt >> 1) * 16 + (lane < 14 ? lane : 0);
  u32x2 fh; gld_b64_sc1(fh, p.ctl, (unsigned)hidx * 8);
  WAITV(0); PIN(fh);
  BAR();
#pragma unroll
  for (int ks = 0; ks < 14; ++ks) afr[14 + ks] = ldstr(lds + wdr + ks * 2048, lds + wdr + ks * 2048 + 512);
  LAS char* rb = lds + C_RED + cl * 24576;
  f32x4 own0, own1;
#pragma unroll
  for (int d = 0; d < 4; ++d) {
    const f32x16& a = (d >> 1) ? y1 : y0;
    const int r0 = 8 * (d & 1);
    const f32x4 v0 = {a[r0], a[r0 + 1], a[r0 + 2], a[r0 + 3]}, v1 = {a[r0 + 4], a[r0 + 5], a[r0 + 6], a[r0 + 7]};
    if (d == wq) { own0 = v0; own1 = v1; }
    else {
      const int sl = wq - (wq > d ? 1 : 0);
      *(LAS f32x4*)(rb + ((d * 3 + sl) * 2 + 0) * 1024 + lane * 16) = v0;
      *(LAS f32x4*)(rb + ((d * 3 + sl) * 2 + 1) * 1024 + lane * 16) = v1;
    }
  }
  BAR();
  STAMP(9);
  if (!tag_ok(fh, tag)) wait_flags(p.ctl, hidx, tag);
  STAMP(10);
  const char* hs = (const char*)p.hfrag + ((size_t)(2 * dtt + dt2) * 112 + 28 * g) * 1024;
  auto stage_h = [&](int rd, int n) {
    const unsigned dst = ldsa(lds) + (rd % 3) * D_SLOT + g * 12288 + dt2 * 1024;
    const int k0 = rd < 3 ? 6 * rd : 18 + 5 * (rd - 3);
#pragma unroll
    for (int i = 0; i < 6; ++i) if (i < n) glds16_sc1(hs + (k0 + i) * 1024, lane * 16, dst + i * 2048);
  };
  stage_h(0, 6); stage_h(1, 6);
  {
    f32x4 lo, hi;
#pragma unroll
    for (int k = 0; k < 4; ++k) {
      auto r = __builtin_amdgcn_permlane32_swap(f2u(own0[k]), f2u(own1[k]), false, false);
      lo[k] = u2f(r[0]); hi[k] = u2f(r[1]);
    }
#pragma unroll
    for (int s = 0; s < 3; ++s) {
      lo += *(LAS f32x4*)(rb + ((wq * 3 + s) * 2 + h) * 1024 + l31 * 16);
      hi += *(LAS f32x4*)(rb + ((wq * 3 + s) * 2 + h) * 1024 + (l31 + 32) * 16);
    }
    const float dk = bf2f(pDk);
    const float yv[8] = {lo[0], lo[1], lo[2], lo[3], hi[0], hi[1], hi[2], hi[3]};
    const unsigned xv[4] = {xs0[0], xs0[1], xs1[0], xs1[1]};
    u32x4 ov;
#pragma unroll
    for (int k = 0; k < 4; ++k) {
      const unsigned yb = pk2(yv[2 * k], yv[2 * k + 1]), sk = pk2(blo(xv[k]) * dk, bhi(xv[k]) * dk);
      ov[k] = pk2(blo(yb) + blo(sk), bhi(yb) + bhi(sk));
    }
    gst_b128_sc1(p.yT, (unsigned)((ch * L + yt) * 2), ov);
  }
  const int ett = g >> 1, erh = g & 1;
  const int et = 64 * dtt + 32 * ett + l31, ed = 64 * ddt + 32 * dt2 + 16 * erh + 8 * h;
  f32x16 dacc[2] = {};
  auto dround = [&](int rd) {
    const int k0 = rd < 3 ? 6 * rd : 18 + 5 * (rd - 3), n = rd < 3 ? 6 : 5;
    LAS char* sb = lds + (rd % 3) * D_SLOT + g * 12288 + lane * 16;
#pragma unroll
    for (int i = 0; i < 6; ++i) if (i < n) {
      dacc[0] = MFMA32(afr[k0 + i], ldsr128(sb + i * 2048), dacc[0]);
      dacc[1] = MFMA32(afr[k0 + i], ldsr128(sb + i * 2048 + 1024), dacc[1]);
    }
  };
  u32x4 xv; u32x2 fy; unsigned yv[8];
  WAITV(7); BAR();
  STAMP(11);
  stage_h(2, 6); gld_b128(xv, p.x, (unsigned)(et * D + ed) * 2);
  dround(0);
  WAITV(7); BAR();
  if (tid == 0) __hip_atomic_store(p.ctl + F_Y + cb, tag, RLX_AGENT);
  stage_h(3, 5);
  dround(1);
  WAITV(5); BAR();
  STAMP(12);
  stage_h(4, 5);
  const int yidx = F_Y + 32 * ddt + l31;
  gld_b64_sc1(fy, p.ctl, (unsigned)yidx * 8);
  dround(2);
  WAITV(6); BAR();
  dround(3);
  WAITV(0); PIN(fy); PIN(xv); BAR();
  STAMP(13);
  if (!tag_ok(fy, tag)) wait_flags(p.ctl, yidx, tag);
#pragma unroll
  for (int e = 0; e < 8; ++e) gld_u16_sc1(yv[e], p.yT + (size_t)e * L, (unsigned)(ed * L + et) * 2);
  dround(4);
  BAR();
  STAMP(14);
  {
    LAS char* db = lds + dt2 * 24576;
    f32x4 lo, hi;
#pragma unroll
    for (int d = 0; d < 4; ++d) {
      const f32x16& a = dacc[d >> 1];
      const int r0 = 8 * (d & 1);
      const f32x4 v0 = {a[r0], a[r0 + 1], a[r0 + 2], a[r0 + 3]}, v1 = {a[r0 + 4], a[r0 + 5], a[r0 + 6], a[r0 + 7]};
      if (d == g) { lo = v0; hi = v1; }
      else {
        const int sl = g - (g > d ? 1 : 0);
        *(LAS f32x4*)(db + ((d * 3 + sl) * 2 + 0) * 1024 + lane * 16) = v0;
        *(LAS f32x4*)(db + ((d * 3 + sl) * 2 + 1) * 1024 + lane * 16) = v1;
      }
    }
    BAR();
#pragma unroll
    for (int s = 0; s < 3; ++s) {
      lo += *(LAS f32x4*)(db + ((g * 3 + s) * 2 + 0) * 1024 + lane * 16);
      hi += *(LAS f32x4*)(db + ((g * 3 + s) * 2 + 1) * 1024 + lane * 16);
    }
    WAITV(0);
#pragma unroll
    for (int e = 0; e < 8; ++e) PIN(yv[e]);
    const float gl[8] = {lo[0], lo[1], lo[2], lo[3], hi[0], hi[1], hi[2], hi[3]};
    u32x4 ov;
#pragma unroll
    for (int k = 0; k < 4; ++k) {
      const unsigned a = pk2(blo(xv[k]) + bf2f(yv[2 * k]), bhi(xv[k]) + bf2f(yv[2 * k + 1]));
      const unsigned b = pk2(gl[2 * k], gl[2 * k + 1]);
      ov[k] = pk2(blo(a) + blo(b), bhi(a) + bhi(b));
    }
    *(u32x4*)(p.out + (size_t)et * D + ed) = ov;
  }
  STAMP(15);
}

extern __shared__ __attribute__((aligned(16))) char smem[];
__global__ __launch_bounds__(512, 2) void k_fused(Params p) {
  const int xcd = blockIdx.x & 7, o = blockIdx.x >> 3;
  if (o < 28) main_block(p, xcd, o, (LAS char*)smem);
  else spare_block(p, xcd * 4 + (o - 28), (LAS char*)smem);
}

#if defined(__HIP_DEVICE_COMPILE__)
#pragma clang attribute pop
#endif

extern "C" void kernel_launch(void* const* d_in, const int* in_sizes, int n_in, void* d_out, int out_size, void* d_ws, size_t ws_size, hipStream_t stream) {
  Params p;
  p.x = (const u16*)d_in[0]; p.Are = (const u16*)d_in[1]; p.Aim = (const u16*)d_in[2]; p.Bre = (const u16*)d_in[3]; p.Bim = (const u16*)d_in[4];
  p.Cre = (const u16*)d_in[5]; p.Cim = (const u16*)d_in[6]; p.logdt = (const u16*)d_in[7]; p.dskip = (const u16*)d_in[8];
  p.wg = (const u16*)d_in[10]; p.wu = (const u16*)d_in[11]; p.wd = (const u16*)d_in[12];
  p.out = (u16*)d_out;
  char* ws = (char*)d_ws;
  p.ctl = (u64*)ws; p.stats = (u64*)(ws + (64 << 10)); p.cs = (float*)(ws + (128 << 10));
  p.hfrag = (u16*)(ws + (1 << 20)); p.xnT = (u16*)(ws + (9 << 20)); p.yT = (u16*)(ws + (11 << 20));
  (void)hipFuncSetAttribute((const void*)k_fused, hipFuncAttributeMaxDynamicSharedMemorySize, LDS_BYTES);
  k_fused<<<256, 512, LDS_BYTES, stream>>>(p);
}
```

```cpp
#define PF_CODE 26
#include <hip/hip_runtime.h>
#include <hip/hip_bf16.h>
#include <stdint.h>

typedef unsigned short u16;
typedef unsigned long long u64;
typedef short bf16x8 __attribute__((ext_vector_type(8)));
typedef short s16x4 __attribute__((ext_vector_type(4)));
typedef __bf16 bf16x2 __attribute__((ext_vector_type(2)));
typedef float f32x2 __attribute__((ext_vector_type(2)));
typedef float f32x4 __attribute__((ext_vector_type(4)));
typedef float f32x16 __attribute__((ext_vector_type(16)));
typedef unsigned u32x4 __attribute__((ext_vector_type(4)));
typedef unsigned u32x2 __attribute__((ext_vector_type(2)));
#define LAS __attribute__((address_space(3)))
#define DI __device__ __forceinline__
#define MFMA32(a, b, c) __builtin_amdgcn_mfma_f32_32x32x16_bf16((a), (b), (c), 0, 0, 0)
#define MFMA16(a, b, c) __builtin_amdgcn_mfma_f32_16x16x32_bf16((a), (b), (c), 0, 0, 0)
#define WAITV(n) asm volatile("s_waitcnt vmcnt(" #n ")" ::: "memory")
#define PIN(v) asm volatile("" : "+v"(v))
#define SB() __builtin_amdgcn_sched_barrier(0)
#define BAR() do { asm volatile("s_waitcnt lgkmcnt(0)" ::: "memory"); __builtin_amdgcn_s_barrier(); asm volatile("" ::: "memory"); } while (0)
#define RLX_AGENT __ATOMIC_RELAXED, __HIP_MEMORY_SCOPE_AGENT

constexpr int L = 2048, D = 448, NS = 256, DF = 1792;

#if defined(__HIP_DEVICE_COMPILE__)
#pragma clang attribute push(__attribute__((target("no-packed-fp32-ops"))), apply_to = function)
#endif
DI float u2f(unsigned u) { return __builtin_bit_cast(float, u); }
DI unsigned f2u(float f) { return __builtin_bit_cast(unsigned, f); }
DI bool wall(bool c) { return __builtin_amdgcn_ballot_w64(c) == __builtin_amdgcn_read_exec(); }

DI float bf2f(unsigned v) { return u2f(v << 16); }
DI unsigned pk2(float lo, float hi) { f32x2 p = {lo, hi}; return __builtin_bit_cast(unsigned, __builtin_convertvector(p, bf16x2)); }
DI float rbf(float f) { return u2f(pk2(f, 0.f) << 16); }
DI float blo(unsigned u) { return u2f(u << 16); }
DI float bhi(unsigned u) { return u2f(u & 0xffff0000u); }
DI unsigned ldsa(LAS char* l) { return (unsigned)(size_t)l; }
DI void glds16(const void* sbase, unsigned voff, unsigned ldst) {
  asm volatile("s_mov_b32 m0, %2\n\ts_nop 0\n\tglobal_load_lds_dwordx4 %0, %1" ::"v"(voff), "s"(sbase), "s"(ldst) : "memory");
}
DI void glds16_sc1(const void* sbase, unsigned voff, unsigned ldst) {
  asm volatile("s_mov_b32 m0, %2\n\ts_nop 0\n\tglobal_load_lds_dwordx4 %0, %1 sc1" ::"v"(voff), "s"(sbase), "s"(ldst) : "memory");
}
#define GLD(name, T, ins) DI void name(T& d, const void* sbase, unsigned voff) { asm volatile(ins : "=v"(d) : "v"(voff), "s"(sbase) : "memory"); }
GLD(gld_u16, unsigned, "global_load_ushort %0, %1, %2")
GLD(gld_u16_sc1, unsigned, "global_load_ushort %0, %1, %2 sc1")
GLD(gld_b32_sc1, unsigned, "global_load_dword %0, %1, %2 sc1")
GLD(gld_b64_sc1, u32x2, "global_load_dwordx2 %0, %1, %2 sc1")
GLD(gld_b128, u32x4, "global_load_dwordx4 %0, %1, %2")
GLD(gld_b128_sc1, u32x4, "global_load_dwordx4 %0, %1, %2 sc1")
DI void gst_b128_sc1(void* sbase, unsigned voff, u32x4 v) {
  asm volatile("global_store_dwordx4 %0, %1, %2 sc1\n\ts_nop 1" ::"v"(voff), "v"(v), "s"(sbase) : "memory");
}
DI bf16x8 ldsr128(LAS char* p) { return *(LAS bf16x8*)p; }
DI bf16x8 ldsr64x2(LAS char* p0, LAS char* p1) {
  s16x4 a = *(LAS s16x4*)p0, b = *(LAS s16x4*)p1;
  return __builtin_shufflevector(a, b, 0, 1, 2, 3, 4, 5, 6, 7);
}
DI bf16x8 ldstr(LAS char* p0, LAS char* p1) {
  s16x4 a = __builtin_amdgcn_ds_read_tr16_b64_v4i16((LAS s16x4*)p0);
  s16x4 b = __builtin_amdgcn_ds_read_tr16_b64_v4i16((LAS s16x4*)p1);
  return __builtin_shufflevector(a, b, 0, 1, 2, 3, 4, 5, 6, 7);
}
template <int CTRL> DI float dppf(float v) { return __builtin_bit_cast(float, __builtin_amdgcn_update_dpp(0, __builtin_bit_cast(int, v), CTRL, 0xf, 0xf, true)); }
DI float red8(float v) {
  v += dppf<0xB1>(v);
  v += dppf<0x4E>(v);
  v += dppf<0x141>(v);
  return v;
}

#define STAMP(i) do {} while (0)

struct Params {
  const u16 *x, *Are, *Aim, *Bre, *Bim, *Cre, *Cim, *logdt, *dskip, *wg, *wu, *wd;
  u16* out;
  u16* hfrag;
  u16* xnT;
  u16* yT;
  u64* stats;
  float* cs;
  u64* ctl;
};
constexpr int F_P1 = 16, F_P2 = 48, F_H = 128, F_Y = 384;
constexpr unsigned SPIN_CAP = 1u << 15;

DI void wait_flags(const u64* ctl, int idx, u64 tag) {
  for (unsigned s = 0; s < SPIN_CAP; ++s) {
    const u64 v = __hip_atomic_load(ctl + idx, RLX_AGENT);
    if (wall(v == tag)) return;
    __builtin_amdgcn_s_sleep(2);
  }
}
DI bool tag_ok(u32x2 v, u64 tag) { return wall(v[0] == (unsigned)tag && v[1] == (unsigned)(tag >> 32)); }

struct KTab { u16 v[2048]; };
constexpr KTab make_ktab() {
  KTab t{};
  for (int i = 0; i < 2048; ++i) {
    const int l = (i & ~7) + 7 - (i & 7);
    int sh = 0;
    while ((l >> sh) >= 256) ++sh;
    int q = l >> sh;
    if (sh) { const int rem = l & ((1 << sh) - 1), half = 1 << (sh - 1); if (rem > half || (rem == half && (q & 1))) ++q; }
    const int v = q << sh;
    int a = 0, b = 0;
    if (v < 256) { a = v & 7; b = v >> 3; }
    else if (v < 512) { a = 8 + ((v & 7) >> 1); b = (v & 255) >> 3; }
    else if (v < 1024) { a = 12 + ((v >> 2) & 1) + 2 * ((v >> 8) & 1); b = (v & 255) >> 3; }
    else if (v < 2048) { a = 16 + ((v >> 8) & 3); b = (v & 255) >> 3; }
    else { a = 20; b = 0; }
    t.v[i] = (u16)((a * 32 + b) * 4);
  }
  return t;
}
__device__ const KTab g_ktab = make_ktab();

struct cf { float r, i; };
DI cf cmul(cf a, cf b) { return {a.r * b.r - a.i * b.i, a.r * b.i + a.i * b.r}; }
DI cf cexp_(float zr2, float zrev, float l) {
  const float m = __builtin_amdgcn_exp2f(zr2 * l), f = __builtin_amdgcn_fractf(zrev * l);
  return {m * __builtin_amdgcn_cosf(f), m * __builtin_amdgcn_sinf(f)};
}

constexpr int P_STRIDE = 928;
DI void spare_block(const Params& p, int sp, LAS char* lds) {
  const int tid = threadIdx.x, lane = tid & 63, w = __builtin_amdgcn_readfirstlane(tid >> 6);
  const int t0 = sp * 64;
  STAMP(0);
  const u64 tag = __hip_atomic_load(p.ctl, RLX_AGENT) + 1;
  const int row = 8 * w + (lane >> 3), pp = lane & 7;
  const u32x4* src = (const u32x4*)(p.x + (size_t)(t0 + row) * D) + pp;
  u32x4 v[7];
#pragma unroll
  for (int i = 0; i < 7; ++i) v[i] = src[8 * i];
  const int js = sp < 28 ? sp : 0;
  const u32x4* wsrc = (const u32x4*)(((js & 1) ? p.wu : p.wg) + (size_t)(tid >> 4) * DF + (js >> 1) * 128) + (tid & 15);
  u32x4 wv[14];
#pragma unroll
  for (int i = 0; i < 14; ++i) wv[i] = wsrc[(size_t)i * (32 * DF / 8)];
  float s = 0.f;
#pragma unroll
  for (int i = 0; i < 7; ++i)
#pragma unroll
    for (int k = 0; k < 4; ++k) s += blo(v[i][k]) + bhi(v[i][k]);
  s = red8(s);
  const float mean = s * (1.f / D);
  float q = 0.f;
#pragma unroll
  for (int i = 0; i < 7; ++i)
#pragma unroll
    for (int k = 0; k < 4; ++k) { float a = blo(v[i][k]) - mean, b = bhi(v[i][k]) - mean; q += a * a + b * b; }
  q = red8(q);
  const float mb = rbf(mean), vb = rbf(q * (1.f / D));
  const float sd = rbf(__builtin_sqrtf(rbf(vb + 1e-5f)));
  const float rs = 1.f / sd;
  if (pp == 0) __hip_atomic_store(p.stats + t0 + row, (u64)f2u(mb) | ((u64)f2u(sd) << 32), RLX_AGENT);
  LAS char* cb = lds + 64 * P_STRIDE;
  {
    f32x4 c0 = {0, 0, 0, 0}, c1 = {0, 0, 0, 0};
#pragma unroll
    for (int i = 0; i < 14; ++i) {
      c0 += (f32x4){blo(wv[i][0]), bhi(wv[i][0]), blo(wv[i][1]), bhi(wv[i][1])};
      c1 += (f32x4){blo(wv[i][2]), bhi(wv[i][2]), blo(wv[i][3]), bhi(wv[i][3])};
    }
    *(LAS f32x4*)(cb + tid * 32) = c0;
    *(LAS f32x4*)(cb + tid * 32 + 16) = c1;
  }
#pragma unroll
  for (int i = 0; i < 7; ++i) {
    u32x4 o;
#pragma unroll
    for (int k = 0; k < 4; ++k) {
      unsigned d = pk2(blo(v[i][k]) - mb, bhi(v[i][k]) - mb);
      o[k] = pk2(blo(d) * rs, bhi(d) * rs);
    }
    *(LAS u32x4*)(lds + row * P_STRIDE + (8 * i + pp) * 16) = o;
  }
  BAR();
  if (tid < 128 && sp < 28) {
    float c = 0.f;
#pragma unroll
    for (int r = 0; r < 32; ++r) c += *(LAS float*)(cb + r * 512 + tid * 4);
    __hip_atomic_store((unsigned*)p.cs + (js & 1) * DF + (js >> 1) * 128 + tid, f2u(c), RLX_AGENT);
  }
  WAITV(0);
  BAR();
  if (tid == 0) __hip_atomic_store(p.ctl + F_P1 + sp, tag, RLX_AGENT);
  STAMP(1);
  const int g16 = lane >> 4, q4 = (lane & 15) >> 2, p4 = lane & 3;
  bf16x8 r[7];
#pragma unroll
  for (int i = 0; i < 7; ++i) {
    const int item = w * 7 + i, ct = item >> 1, rd = item & 1;
    LAS char* a = lds + (32 * rd + 8 * g16 + q4) * P_STRIDE + ct * 32 + p4 * 8;
    r[i] = ldstr(a, a + 4 * P_STRIDE);
  }
#pragma unroll
  for (int i = 0; i < 7; ++i) {
    const int item = w * 7 + i, ct = item >> 1, rd = item & 1;
    gst_b128_sc1(p.xnT, (unsigned)(((ct * 16 + (lane & 15)) * L + t0 + 32 * rd + 8 * g16) * 2), __builtin_bit_cast(u32x4, r[i]));
  }
  WAITV(0);
  BAR();
  if (tid == 0) __hip_atomic_store(p.ctl + F_P2 + sp, tag, RLX_AGENT);
  STAMP(2);
  if (sp == 0 && w == 0) {
    wait_flags(p.ctl, F_P1 + (lane & 31), tag);
#pragma unroll 1
    for (int i = 0; i < 4; ++i) { const int e = lane + 64 * i; wait_flags(p.ctl, F_H + ((e & 15) < 14 ? e : (e & ~15)), tag); }
    if (lane == 0) __hip_atomic_store(p.ctl, tag, RLX_AGENT);
  }
  STAMP(3);
}

constexpr int U_SLOT = 49408;
constexpr int K_SCR = 2 * U_SLOT, K_SW = 16 * 272, K_TAB = K_SCR + 8 * K_SW;
constexpr int C_K = 3 * U_SLOT, C_KCH = 7680;
constexpr int C_X = 114688, C_XC = 4160, C_XCH = 4 * C_XC;
constexpr int C_RED = 114688;
constexpr int D_SLOT = 49152;
constexpr int LDS_BYTES = 163840;
static_assert(C_K + 2 * C_KCH <= LDS_BYTES && C_X + 2 * C_XCH <= C_K && K_TAB + 2 * 672 * 4 <= C_K, "lds map");

DI void main_block(const Params& p, int xcd, int o, LAS char* lds) {
  const int tid = threadIdx.x, lane = tid & 63, w = __builtin_amdgcn_readfirstlane(tid >> 6);
  const int h = lane >> 5, l31 = lane & 31, blk = (lane >> 4) & 1, q4 = (lane & 15) >> 2, p4 = lane & 3;
  const int sg = ((p4 & 1) << 1) | (p4 >> 1);
  const int tt = 4 * (xcd >> 1) + o / 7, fs = 7 * (xcd & 1) + o % 7;
  const int cb = xcd * 28 + o;
  const int dtt = 4 * xcd + o / 7, ddt = o % 7;
  const int cl = w >> 2, wq = w & 3, ch = 2 * cb + cl, tc = tid & 255;
  const int tg = w >> 2, cg = w & 3;
  const int t0 = tt * 128;
  STAMP(0);
  unsigned pAr, pAi, pBr, pBi, pCr, pCi, pDt, pDk; u32x4 kidx; u32x2 ep;
  {
    const unsigned no = (unsigned)(ch * NS + tc) * 2;
    gld_b64_sc1(ep, p.ctl, 0u);
    gld_u16(pDt, p.logdt, (unsigned)ch * 2); gld_u16(pAr, p.Are, no); gld_u16(pAi, p.Aim, no); gld_u16(pBr, p.Bre, no); gld_u16(pBi, p.Bim, no);
    gld_u16(pCr, p.Cre, no); gld_u16(pCi, p.Cim, no); gld_u16(pDk, p.dskip, (unsigned)ch * 2);
    gld_b128(kidx, &g_ktab, (unsigned)tc * 16);
  }
  unsigned xo[2], wo[2];
#pragma unroll
  for (int i = 0; i < 2; ++i) {
    const int r = 8 * (2 * w + i) + (lane >> 3), c = (lane & 7) ^ ((r >> 1) & 7);
    xo[i] = (unsigned)((t0 + r) * (D * 2) + c * 16);
    const int kr = 4 * (2 * w + i) + (lane >> 4), cp = lane & 15;
    const int cc = (cp & 8) | ((((cp >> 1) & 3) ^ (kr & 3)) << 1) | (cp & 1);
    wo[i] = (unsigned)(kr * (DF * 2) + fs * 256 + cc * 16);
  }
  const unsigned lb = ldsa(lds) + w * 2048;
  auto stage_x = [&](int kc, int slot) {
    const unsigned b = lb + slot * U_SLOT;
    const char* xs = (const char*)p.x + kc * 128;
    glds16(xs, xo[0], b); glds16(xs, xo[1], b + 1024);
  };
  auto stage_g = [&](int kc, int slot) {
    const unsigned b = lb + slot * U_SLOT + 16384;
    const char* gs = (const char*)p.wg + (size_t)kc * (64 * DF * 2);
    glds16(gs, wo[0], b); glds16(gs, wo[1], b + 1024);
  };
  auto stage_u = [&](int kc, int slot) {
    const unsigned b = lb + slot * U_SLOT + 32768 + 128;
    const char* us = (const char*)p.wu + (size_t)kc * (64 * DF * 2);
    glds16(us, wo[0], b); glds16(us, wo[1], b + 1024);
  };
#pragma unroll
  for (int c = 0; c < 2; ++c) { stage_x(c, c); stage_g(c, c); stage_u(c, c); }
  if (tc < 160) *(LAS u32x4*)(lds + C_K + cl * C_KCH + tc * 16) = (u32x4){0, 0, 0, 0};
  WAITV(12);
  PIN(ep); PIN(pDt); PIN(pAr); PIN(pAi); PIN(pBr); PIN(pBi); PIN(pCr); PIN(pCi); PIN(pDk); PIN(kidx);
  const u64 tag = (((u64)__builtin_amdgcn_readfirstlane(ep[1]) << 32) | (unsigned)__builtin_amdgcn_readfirstlane(ep[0])) + 1;
  STAMP(1);
  {
    const float dt = rbf(__builtin_amdgcn_exp2f(bf2f(pDt) * 1.4426950408889634f));
    const float zr2 = dt * bf2f(pAr) * 1.4426950408889634f, zrev = dt * bf2f(pAi) * 0.15915494309189535f;
    const cf cc = cmul({bf2f(pCr), bf2f(pCi)}, {dt * bf2f(pBr), dt * bf2f(pBi)});
    LAS char* ew = lds + K_SCR + w * K_SW;
    LAS char* ewr = ew + lane * 4;
    LAS char* erd = ew + (lane & 15) * 272 + (lane >> 4) * 16;
    bf16x8 bfr[2][4];
    {
      cf e[16];
      e[0] = {1.f, 0.f};
      e[1] = cexp_(zr2, zrev, 8.f);
      e[2] = cmul(e[1], e[1]); e[3] = cmul(e[2], e[1]);
      e[4] = cexp_(zr2, zrev, 32.f);
#pragma unroll
      for (int b = 5; b < 8; ++b) e[b] = cmul(e[4], e[b - 4]);
      e[8] = cmul(e[4], e[4]);
#pragma unroll
      for (int b = 9; b < 16; ++b) e[b] = cmul(e[8], e[b - 8]);
#pragma unroll
      for (int b = 0; b < 16; ++b) *(LAS unsigned*)(ewr + b * 272) = pk2(e[b].r, e[b].i);
#pragma unroll
      for (int s = 0; s < 4; ++s) bfr[0][s] = ldsr128(erd + s * 64);
      const cf e16 = cexp_(zr2, zrev, 128.f);
#pragma unroll
      for (int b = 0; b < 16; ++b) { const cf t = cmul(e16, e[b]); *(LAS unsigned*)(ewr + b * 272) = pk2(t.r, t.i); }
#pragma unroll
      for (int s = 0; s < 4; ++s) bfr[1][s] = ldsr128(erd + s * 64);
    }
    f32x4 kt[2][2] = {};
    {
      cf e[16];
      const cf p1 = cexp_(zr2, zrev, 1.f), p2 = cmul(p1, p1), pq = cmul(p2, p2);
      e[0] = cc; e[1] = cmul(cc, p1); e[2] = cmul(cc, p2); e[3] = cmul(e[2], p1);
      e[4] = cmul(cc, pq); e[5] = cmul(e[4], p1); e[6] = cmul(e[4], p2); e[7] = cmul(e[6], p1);
      const cf q1 = cexp_(zr2, zrev, 256.f), q2 = cexp_(zr2, zrev, 512.f), q3 = cmul(q2, q1), q4c = cexp_(zr2, zrev, 1024.f);
      e[8] = cmul(e[0], q1); e[9] = cmul(e[2], q1); e[10] = cmul(e[4], q1); e[11] = cmul(e[6], q1);
      e[12] = cmul(e[0], q2); e[13] = cmul(e[4], q2); e[14] = cmul(e[0], q3); e[15] = cmul(e[4], q3);
#pragma unroll
      for (int a = 0; a < 16; ++a) *(LAS unsigned*)(ewr + a * 272) = pk2(e[a].r, -e[a].i);
#pragma unroll
      for (int s = 0; s < 4; ++s) {
        const bf16x8 a = ldsr128(erd + s * 64);
        kt[0][0] = MFMA16(a, bfr[0][s], kt[0][0]); kt[0][1] = MFMA16(a, bfr[1][s], kt[0][1]);
      }
      cf f[5];
      f[0] = cmul(e[0], q4c); f[1] = cmul(e[8], q4c); f[2] = cmul(e[12], q4c); f[3] = cmul(e[14], q4c); f[4] = cmul(f[0], q4c);
#pragma unroll
      for (int a = 0; a < 5; ++a) *(LAS unsigned*)(ewr + a * 272) = pk2(f[a].r, -f[a].i);
#pragma unroll
      for (int s = 0; s < 4; ++s) {
        const bf16x8 a = ldsr128(erd + s * 64);
        kt[1][0] = MFMA16(a, bfr[0][s], kt[1][0]); kt[1][1] = MFMA16(a, bfr[1][s], kt[1][1]);
      }
    }
    LAS char* pw = ew + (4 * (lane >> 4)) * 128 + (lane & 15) * 4;
#pragma unroll
    for (int r = 0; r < 4; ++r) { *(LAS float*)(pw + r * 128) = kt[0][0][r]; *(LAS float*)(pw + r * 128 + 64) = kt[0][1][r]; }
    if (lane < 32) {
#pragma unroll
      for (int r = 0; r < 4; ++r) { *(LAS float*)(pw + 2048 + r * 128) = kt[1][0][r]; *(LAS float*)(pw + 2048 + r * 128 + 64) = kt[1][1][r]; }
    }
    STAMP(2);
    BAR();
    LAS char* tab = lds + K_TAB + cl * (672 * 4);
    LAS char* pb = lds + K_SCR + cl * 4 * K_SW + tc * 4;
#pragma unroll
    for (int i = 0; i < 3; ++i) {
      if (i < 2 || tc < 160) {
        float s = *(LAS float*)(pb + i * 1024) + *(LAS float*)(pb + K_SW + i * 1024);
        s += *(LAS float*)(pb + 2 * K_SW + i * 1024) + *(LAS float*)(pb + 3 * K_SW + i * 1024);
        *(LAS float*)(tab + tc * 4 + i * 1024) = s;
      }
    }
    BAR();
    u32x4 ko;
#pragma unroll
    for (int k = 0; k < 4; ++k) ko[k] = pk2(*(LAS float*)(tab + (kidx[k] & 0xffffu)), *(LAS float*)(tab + (kidx[k] >> 16)));
    *(LAS u32x4*)(lds + C_K + cl * C_KCH + (32 + (tc >> 2)) * 80 + (tc & 3) * 16) = ko;
  }
  int aoff[2], boff[4];
#pragma unroll
  for (int i = 0; i < 2; ++i) {
    const int a = 2 * cg + i;
    aoff[i] = 16384 + blk * (16384 + 128) + (8 * h + q4) * 256 + ((a >> 2) << 7) + (((a & 3) ^ q4) << 5) + sg * 8;
  }
  const int brow = 64 * tg + l31;
#pragma unroll
  for (int s = 0; s < 4; ++s) boff[s] = brow * 128 + (((2 * s + h) ^ ((brow >> 1) & 7)) << 4);
  f32x16 acc[2][2] = {};
  PIN(acc[0][0]); PIN(acc[0][1]); PIN(acc[1][0]); PIN(acc[1][1]);
  struct Frag { bf16x8 a0, a1, b0, b1; };
  auto ldfrag = [&](LAS char* sb, int s) {
    Frag f;
    f.b0 = ldsr128(sb + boff[s]);
    f.a0 = ldstr(sb + aoff[0] + s * 4096, sb + aoff[0] + s * 4096 + 1024);
    f.b1 = ldsr128(sb + boff[s] + 4096);
    f.a1 = ldstr(sb + aoff[1] + s * 4096, sb + aoff[1] + s * 4096 + 1024);
    return f;
  };
  auto mm = [&](const Frag& f) {
    acc[0][0] = MFMA32(f.a0, f.b0, acc[0][0]);
    acc[1][0] = MFMA32(f.a0, f.b1, acc[1][0]);
    acc[0][1] = MFMA32(f.a1, f.b0, acc[0][1]);
    acc[1][1] = MFMA32(f.a1, f.b1, acc[1][1]);
  };
  WAITV(6);
  BAR();
  STAMP(3);
  stage_x(2, 2); stage_g(2, 2); stage_u(2, 2);
  Frag f0 = ldfrag(lds, 0), f1;
  auto body3 = [&](LAS char* sb) {
    f1 = ldfrag(sb, 1); SB(); mm(f0); SB();
    f0 = ldfrag(sb, 2); SB(); mm(f1); SB();
    f1 = ldfrag(sb, 3); SB(); mm(f0); SB();
  };
  int slot = 0;
#pragma unroll 1
  for (int c = 0; c < 4; ++c) {
    body3(lds + slot * U_SLOT);
    const int nslot = slot == 2 ? 0 : slot + 1;
    WAITV(6);
    BAR();
    f0 = ldfrag(lds + nslot * U_SLOT, 0);
    SB();
    acc[0][0] = MFMA32(f1.a0, f1.b0, acc[0][0]); SB();
    stage_x(c + 3, slot); SB();
    acc[1][0] = MFMA32(f1.a0, f1.b1, acc[1][0]); SB();
    stage_g(c + 3, slot); SB();
    acc[0][1] = MFMA32(f1.a1, f1.b0, acc[0][1]); SB();
    stage_u(c + 3, slot); SB();
    acc[1][1] = MFMA32(f1.a1, f1.b1, acc[1][1]); SB();
    slot = nslot;
  }
  STAMP(4);
  body3(lds + 1 * U_SLOT);
  WAITV(6);
  BAR();
  f0 = ldfrag(lds + 2 * U_SLOT, 0); SB();
  const int pidx = lane >= 32 ? F_P2 + l31 : F_P1 + ((lane & 2) ? 2 * fs : 2 * tt) + (lane & 1);
  u32x2 fl; gld_b64_sc1(fl, p.ctl, (unsigned)pidx * 8); SB();
  mm(f1); SB();
  body3(lds + 2 * U_SLOT);
  WAITV(0); PIN(fl);
  BAR();
  f0 = ldfrag(lds, 0); SB();
  if (!tag_ok(fl, tag)) wait_flags(p.ctl, pidx, tag);
  u32x2 st[2]; unsigned csv[2]; u32x4 xa, xb;
  {
    gld_b64_sc1(st[0], p.stats, (unsigned)(t0 + brow) * 8); gld_b64_sc1(st[1], p.stats, (unsigned)(t0 + brow + 32) * 8);
    const int r16 = lane & 15, fo = 128 * fs + 32 * cg + 4 * (((r16 >> 2 & 1) << 1) | (r16 >> 3)) + (r16 & 3);
    gld_b32_sc1(csv[0], p.cs, (unsigned)(blk * DF + fo) * 4); gld_b32_sc1(csv[1], p.cs, (unsigned)(blk * DF + fo + 16) * 4);
    const int ia = (tc + 252) & 255;
    gld_b128_sc1(xa, p.xnT, (unsigned)(ch * L * 2 + ia * 16)); gld_b128_sc1(xb, p.xnT, (unsigned)(ch * L * 2 + ((ia + 1) & 255) * 16));
  }
  SB();
  mm(f1); SB();
  body3(lds);
  BAR();
  STAMP(5);
  const int dt2 = w & 1, g = w >> 1;
  unsigned wdo;
  { const int r8 = lane >> 3, cc = (lane & 7) ^ (((r8 >> 1) & 1) << 2); wdo = (unsigned)((448 * g + 112 * dt2 + r8) * (D * 2) + ddt * 128 + cc * 16); }
  const int wdr = g * 28672 + (8 * h + q4) * 128 + ((dt2 ^ ((q4 >> 1) & 1)) << 6) + blk * 32 + sg * 8;
  auto stage_wd = [&](int rd) {
    const char* src = (const char*)p.wd + (size_t)rd * (224 * D * 2);
    const unsigned dst = ldsa(lds) + g * 28672 + dt2 * 14336;
#pragma unroll
    for (int i = 0; i < 14; ++i) glds16(src + i * (8 * D * 2), wdo, dst + i * 1024);
  };
  acc[0][0] = MFMA32(f1.a0, f1.b0, acc[0][0]); acc[1][0] = MFMA32(f1.a0, f1.b1, acc[1][0]); SB();
  stage_wd(0); SB();
  acc[0][1] = MFMA32(f1.a1, f1.b0, acc[0][1]); acc[1][1] = MFMA32(f1.a1, f1.b1, acc[1][1]); SB();
  WAITV(14);
  PIN(st[0]); PIN(st[1]); PIN(csv[0]); PIN(csv[1]); PIN(xa); PIN(xb);
  {
    bf16x8 ca[2];
#pragma unroll
    for (int i = 0; i < 2; ++i) {
      const float c = h ? 0.f : u2f(csv[i]);
      const float c0 = rbf(c), r1 = c - c0, c1 = rbf(r1), c2 = r1 - c1;
      u32x4 t = {pk2(c0, c1), pk2(c2, 0.f), 0u, 0u};
      ca[i] = __builtin_bit_cast(bf16x8, t);
    }
#pragma unroll
    for (int j = 0; j < 2; ++j) {
      const unsigned nm = (st[j][0] >> 16) ^ 0x8000u;
      u32x4 t = {nm | (nm << 16), nm, 0u, 0u};
      bf16x8 mb = __builtin_bit_cast(bf16x8, t);
      acc[j][0] = MFMA32(ca[0], mb, acc[j][0]);
      acc[j][1] = MFMA32(ca[1], mb, acc[j][1]);
    }
    const float K1 = -2.f * 0.7978845608028654f * 1.4426950408889634f, K2 = K1 * 0.044715f;
#pragma unroll
    for (int j = 0; j < 2; ++j) {
      const float sd = u2f(st[j][1]), r = 1.f / sd, a1 = K1 * r, a2 = K2 * r * r * r, s2 = sd * sd;
#pragma unroll
      for (int i = 0; i < 2; ++i) {
        float hv[8];
#pragma unroll
        for (int e = 0; e < 8; ++e) {
          const float gg = acc[j][i][e], u = acc[j][i][e + 8];
          const float ex = __builtin_amdgcn_exp2f(gg * __builtin_fmaf(gg * gg, a2, a1));
          hv[e] = (gg * u) * __builtin_amdgcn_rcpf(__builtin_fmaf(ex, s2, s2));
        }
        u32x4 ov = {pk2(hv[0], hv[1]), pk2(hv[2], hv[3]), pk2(hv[4], hv[5]), pk2(hv[6], hv[7])};
        const int tile = 4 * tt + 2 * tg + j, fstep = 8 * fs + 2 * cg + i;
        gst_b128_sc1(p.hfrag, (unsigned)(((tile * 112 + fstep) * 64 + lane) * 16), ov);
      }
    }
  }
  STAMP(6);
  {
    LAS char* xw = lds + C_X + cl * C_XCH;
    auto build = [&](const u32x4& A, const u32x4& B, int oct) {
      const unsigned o0 = __builtin_amdgcn_alignbit(A[1], A[0], 16), o1 = __builtin_amdgcn_alignbit(A[2], A[1], 16), o2 = __builtin_amdgcn_alignbit(A[3], A[2], 16);
      const unsigned o3 = __builtin_amdgcn_alignbit(B[0], A[3], 16), o4 = __builtin_amdgcn_alignbit(B[1], B[0], 16);
      *(LAS u32x4*)(xw + 0 * C_XC + oct * 16) = (u32x4){o0, o1, o2, o3};
      *(LAS u32x4*)(xw + 1 * C_XC + oct * 16) = (u32x4){A[1], A[2], A[3], B[0]};
      *(LAS u32x4*)(xw + 2 * C_XC + oct * 16) = (u32x4){o1, o2, o3, o4};
      *(LAS u32x4*)(xw + 3 * C_XC + oct * 16) = (u32x4){A[2], A[3], B[0], B[1]};
    };
    const u32x4 z4 = {0, 0, 0, 0};
    build(xa, tc == 3 ? z4 : xb, tc >= 4 ? tc : 256 + tc);
    if (tc < 4) build(z4, tc == 3 ? xb : z4, tc);
  }
  WAITV(0);
  BAR();
  if (tid == 0) __hip_atomic_store(p.ctl + F_H + tt * 16 + fs, tag, RLX_AGENT);
  STAMP(7);
  bf16x8 afr[28];
#pragma unroll
  for (int ks = 0; ks < 14; ++ks) afr[ks] = ldstr(lds + wdr + ks * 2048, lds + wdr + ks * 2048 + 512);
  f32x16 y0 = {}, y1 = {};
  {
    LAS char* ap = lds + C_X + cl * C_XCH + (l31 & 3) * C_XC + 48 + 8 * (l31 >> 2) - 16 * h + 64 * (8 * wq);
    LAS char* ap2 = ap + 8;
    PIN(ap2);
    LAS char* bp = lds + C_K + cl * C_KCH + (32 + l31 - 8 * wq) * 80 + 16 * h;
    auto step = [&]() {
#pragma unroll
      for (int s = 0; s < 2; ++s) {
        bf16x8 a0 = ldsr64x2(ap - 32 * s, ap2 - 32 * s), a1 = ldsr64x2(ap - 32 * s + 2048, ap2 - 32 * s + 2048);
        bf16x8 b0 = ldsr128(bp + 32 * s), b1 = ldsr128(bp + 32 * s + 2560);
        y0 = MFMA32(a0, b0, y0);
        y1 = MFMA32(a1, b0, y1);
        y1 = MFMA32(a0, b1, y1);
      }
      ap += 64; ap2 += 64; bp -= 80;
    };
    step();
    BAR();
    stage_wd(1);
#pragma unroll 1
    for (int u = 1; u < 8; ++u) step();
  }
  STAMP(8);
  const int rc = wq >> 1, rg = 2 * (wq & 1) + h;
  const int yt = 32 * (32 * rc + l31) + 8 * rg;
  LAS char* xsk = lds + C_X + cl * C_XCH + 3 * C_XC + (yt + 28) * 2;
  u32x2 xs0 = *(LAS u32x2*)xsk, xs1 = *(LAS u32x2*)(xsk + 8);
  const int hidx = F_H + (dtt >> 1) * 16 + (lane < 14 ? lane : 0);
  u32x2 fh; gld_b64_sc1(fh, p.ctl, (unsigned)hidx * 8);
  WAITV(0); PIN(fh);
  BAR();
#pragma unroll
  for (int ks = 0; ks < 14; ++ks) afr[14 + ks] = ldstr(lds + wdr + ks * 2048, lds + wdr + ks * 2048 + 512);
  LAS char* rb = lds + C_RED + cl * 24576;
  f32x4 own0, own1;
#pragma unroll
  for (int d = 0; d < 4; ++d) {
    const f32x16& a = (d >> 1) ? y1 : y0;
    const int r0 = 8 * (d & 1);
    const f32x4 v0 = {a[r0], a[r0 + 1], a[r0 + 2], a[r0 + 3]}, v1 = {a[r0 + 4], a[r0 + 5], a[r0 + 6], a[r0 + 7]};
    if (d == wq) { own0 = v0; own1 = v1; }
    else {
      const int sl = wq - (wq > d ? 1 : 0);
      *(LAS f32x4*)(rb + ((d * 3 + sl) * 2 + 0) * 1024 + lane * 16) = v0;
      *(LAS f32x4*)(rb + ((d * 3 + sl) * 2 + 1) * 1024 + lane * 16) = v1;
    }
  }
  BAR();
  STAMP(9);
  if (!tag_ok(fh, tag)) wait_flags(p.ctl, hidx, tag);
  STAMP(10);
  const char* hs = (const char*)p.hfrag + ((size_t)(2 * dtt + dt2) * 112 + 28 * g) * 1024;
  auto stage_h = [&](int rd, int n) {
    const unsigned dst = ldsa(lds) + (rd % 3) * D_SLOT + g * 12288 + dt2 * 1024;
    const int k0 = rd < 3 ? 6 * rd : 18 + 5 * (rd - 3);
#pragma unroll
    for (int i = 0; i < 6; ++i) if (i < n) glds16_sc1(hs + (k0 + i) * 1024, lane * 16, dst + i * 2048);
  };
  stage_h(0, 6); stage_h(1, 6);
  {
    f32x4 lo, hi;
#pragma unroll
    for (int k = 0; k < 4; ++k) {
      auto r = __builtin_amdgcn_permlane32_swap(f2u(own0[k]), f2u(own1[k]), false, false);
      lo[k] = u2f(r[0]); hi[k] = u2f(r[1]);
    }
#pragma unroll
    for (int s = 0; s < 3; ++s) {
      lo += *(LAS f32x4*)(rb + ((wq * 3 + s) * 2 + h) * 1024 + l31 * 16);
      hi += *(LAS f32x4*)(rb + ((wq * 3 + s) * 2 + h) * 1024 + (l31 + 32) * 16);
    }
    const float dk = bf2f(pDk);
    const float yv[8] = {lo[0], lo[1], lo[2], lo[3], hi[0], hi[1], hi[2], hi[3]};
    const unsigned xv[4] = {xs0[0], xs0[1], xs1[0], xs1[1]};
    u32x4 ov;
#pragma unroll
    for (int k = 0; k < 4; ++k) {
      const unsigned yb = pk2(yv[2 * k], yv[2 * k + 1]), sk = pk2(blo(xv[k]) * dk, bhi(xv[k]) * dk);
      ov[k] = pk2(blo(yb) + blo(sk), bhi(yb) + bhi(sk));
    }
    gst_b128_sc1(p.yT, (unsigned)((ch * L + yt) * 2), ov);
  }
  const int ett = g >> 1, erh = g & 1;
  const int et = 64 * dtt + 32 * ett + l31, ed = 64 * ddt + 32 * dt2 + 16 * erh + 8 * h;
  f32x16 dacc[2] = {};
  auto dround = [&](int rd) {
    const int k0 = rd < 3 ? 6 * rd : 18 + 5 * (rd - 3), n = rd < 3 ? 6 : 5;
    LAS char* sb = lds + (rd % 3) * D_SLOT + g * 12288 + lane * 16;
#pragma unroll
    for (int i = 0; i < 6; ++i) if (i < n) {
      dacc[0] = MFMA32(afr[k0 + i], ldsr128(sb + i * 2048), dacc[0]);
      dacc[1] = MFMA32(afr[k0 + i], ldsr128(sb + i * 2048 + 1024), dacc[1]);
    }
  };
  u32x4 xv; u32x2 fy; unsigned yv[8];
  WAITV(7); BAR();
  STAMP(11);
  stage_h(2, 6); gld_b128(xv, p.x, (unsigned)(et * D + ed) * 2);
  dround(0);
  WAITV(7); BAR();
  if (tid == 0) __hip_atomic_store(p.ctl + F_Y + cb, tag, RLX_AGENT);
  stage_h(3, 5);
  dround(1);
  WAITV(5); BAR();
  STAMP(12);
  stage_h(4, 5);
  const int yidx = F_Y + 32 * ddt + l31;
  gld_b64_sc1(fy, p.ctl, (unsigned)yidx * 8);
  dround(2);
  WAITV(6); BAR();
  dround(3);
  WAITV(0); PIN(fy); PIN(xv); BAR();
  STAMP(13);
  if (!tag_ok(fy, tag)) wait_flags(p.ctl, yidx, tag);
#pragma unroll
  for (int e = 0; e < 8; ++e) gld_u16_sc1(yv[e], p.yT + (size_t)e * L, (unsigned)(ed * L + et) * 2);
  dround(4);
  BAR();
  STAMP(14);
  {
    LAS char* db = lds + dt2 * 24576;
    f32x4 lo, hi;
#pragma unroll
    for (int d = 0; d < 4; ++d) {
      const f32x16& a = dacc[d >> 1];
      const int r0 = 8 * (d & 1);
      const f32x4 v0 = {a[r0], a[r0 + 1], a[r0 + 2], a[r0 + 3]}, v1 = {a[r0 + 4], a[r0 + 5], a[r0 + 6], a[r0 + 7]};
      if (d == g) { lo = v0; hi = v1; }
      else {
        const int sl = g - (g > d ? 1 : 0);
        *(LAS f32x4*)(db + ((d * 3 + sl) * 2 + 0) * 1024 + lane * 16) = v0;
        *(LAS f32x4*)(db + ((d * 3 + sl) * 2 + 1) * 1024 + lane * 16) = v1;
      }
    }
    BAR();
#pragma unroll
    for (int s = 0; s < 3; ++s) {
      lo += *(LAS f32x4*)(db + ((g * 3 + s) * 2 + 0) * 1024 + lane * 16);
      hi += *(LAS f32x4*)(db + ((g * 3 + s) * 2 + 1) * 1024 + lane * 16);
    }
    WAITV(0);
#pragma unroll
    for (int e = 0; e < 8; ++e) PIN(yv[e]);
    const float gl[8] = {lo[0], lo[1], lo[2], lo[3], hi[0], hi[1], hi[2], hi[3]};
    u32x4 ov;
#pragma unroll
    for (int k = 0; k < 4; ++k) {
      const unsigned a = pk2(blo(xv[k]) + bf2f(yv[2 * k]), bhi(xv[k]) + bf2f(yv[2 * k + 1]));
      const unsigned b = pk2(gl[2 * k], gl[2 * k + 1]);
      ov[k] = pk2(blo(a) + blo(b), bhi(a) + bhi(b));
    }
    *(u32x4*)(p.out + (size_t)et * D + ed) = ov;
  }
  STAMP(15);
}

extern __shared__ __attribute__((aligned(16))) char smem[];
__global__ __launch_bounds__(512, 2) void k_fused(Params p) {
  const int xcd = blockIdx.x & 7, o = blockIdx.x >> 3;
  const int w_ = __builtin_amdgcn_readfirstlane(threadIdx.x >> 6);
  const unsigned pdst = ldsa((LAS char*)smem) + C_K + (w_ >> 2) * C_KCH + 2560 + (w_ & 3) * 1024;
  {
    const char* pc; asm volatile("s_getpc_b64 %0" : "=s"(pc));
    glds16((const char*)((size_t)pc & ~(size_t)255), (unsigned)(((o * 8 + w_) % PF_CODE) * 1024 + (threadIdx.x & 63) * 16), pdst);
  }
  if (o < 28) main_block(p, xcd, o, (LAS char*)smem);
  else spare_block(p, xcd * 4 + (o - 28), (LAS char*)smem);
}

#if defined(__HIP_DEVICE_COMPILE__)
#pragma clang attribute pop
#endif

extern "C" void kernel_launch(void* const* d_in, const int* in_sizes, int n_in, void* d_out, int out_size, void* d_ws, size_t ws_size, hipStream_t stream) {
  Params p;
  p.x = (const u16*)d_in[0]; p.Are = (const u16*)d_in[1]; p.Aim = (const u16*)d_in[2]; p.Bre = (const u16*)d_in[3]; p.Bim = (const u16*)d_in[4];
  p.Cre = (const u16*)d_in[5]; p.Cim = (const u16*)d_in[6]; p.logdt = (const u16*)d_in[7]; p.dskip = (const u16*)d_in[8];
  p.wg = (const u16*)d_in[10]; p.wu = (const u16*)d_in[11]; p.wd = (const u16*)d_in[12];
  p.out = (u16*)d_out;
  char* ws = (char*)d_ws;
  p.ctl = (u64*)ws; p.stats = (u64*)(ws + (64 << 10)); p.cs = (float*)(ws + (128 << 10));
  p.hfrag = (u16*)(ws + (1 << 20)); p.xnT = (u16*)(ws + (9 << 20)); p.yT = (u16*)(ws + (11 << 20));
  (void)hipFuncSetAttribute((const void*)k_fused, hipFuncAttributeMaxDynamicSharedMemorySize, LDS_BYTES);
  k_fused<<<256, 512, LDS_BYTES, stream>>>(p);
}
```

```cpp
#include <hip/hip_runtime.h>
#include <hip/hip_bf16.h>
#include <stdint.h>
#include <type_traits>

typedef unsigned short u16;
typedef unsigned long long u64;
typedef short bf16x8 __attribute__((ext_vector_type(8)));
typedef short s16x4 __attribute__((ext_vector_type(4)));
typedef __bf16 bf16x2 __attribute__((ext_vector_type(2)));
typedef float f32x2 __attribute__((ext_vector_type(2)));
typedef float f32x4 __attribute__((ext_vector_type(4)));
typedef float f32x16 __attribute__((ext_vector_type(16)));
typedef unsigned u32x4 __attribute__((ext_vector_type(4)));
typedef unsigned u32x2 __attribute__((ext_vector_type(2)));
#define LAS __attribute__((address_space(3)))
#define DI __device__ __forceinline__
#define MFMA32(a, b, c) __builtin_amdgcn_mfma_f32_32x32x16_bf16((a), (b), (c), 0, 0, 0)
#define MFMA16(a, b, c) __builtin_amdgcn_mfma_f32_16x16x32_bf16((a), (b), (c), 0, 0, 0)
#define WAITV(n) asm volatile("s_waitcnt vmcnt(" #n ")" ::: "memory")
template <int N> __device__ __forceinline__ void waitv() { asm volatile("s_waitcnt vmcnt(%0)" ::"i"(N) : "memory"); }
#define PIN(v) asm volatile("" : "+v"(v))
#define SB() __builtin_amdgcn_sched_barrier(0)
#define BAR() do { asm volatile("s_waitcnt lgkmcnt(0)" ::: "memory"); __builtin_amdgcn_s_barrier(); asm volatile("" ::: "memory"); } while (0)
#define RLX_AGENT __ATOMIC_RELAXED, __HIP_MEMORY_SCOPE_AGENT

constexpr int L = 2048, D = 448, NS = 256, DF = 1792;

#if defined(__HIP_DEVICE_COMPILE__)
#pragma clang attribute push(__attribute__((target("no-packed-fp32-ops"))), apply_to = function)
#endif
template <int I, int N, class F> DI void sfor(F&& f) { if constexpr (I < N) { f(std::integral_constant<int, I>{}); sfor<I + 1, N>(f); } }
DI float u2f(unsigned u) { return __builtin_bit_cast(float, u); }
DI unsigned f2u(float f) { return __builtin_bit_cast(unsigned, f); }
DI bool wall(bool c) { return __builtin_amdgcn_ballot_w64(c) == __builtin_amdgcn_read_exec(); }

DI float bf2f(unsigned v) { return u2f(v << 16); }
DI unsigned pk2(float lo, float hi) { f32x2 p = {lo, hi}; return __builtin_bit_cast(unsigned, __builtin_convertvector(p, bf16x2)); }
DI float rbf(float f) { return u2f(pk2(f, 0.f) << 16); }
DI float blo(unsigned u) { return u2f(u << 16); }
DI float bhi(unsigned u) { return u2f(u & 0xffff0000u); }
DI float dot2(unsigned a, unsigned b, float c) { return __builtin_amdgcn_fdot2_f32_bf16(__builtin_bit_cast(bf16x2, a), __builtin_bit_cast(bf16x2, b), c, false); }
DI unsigned ldsa(LAS char* l) { return (unsigned)(size_t)l; }
DI void glds16(const void* sbase, unsigned voff, unsigned ldst) {
  asm volatile("s_mov_b32 m0, %2\n\ts_nop 0\n\tglobal_load_lds_dwordx4 %0, %1" ::"v"(voff), "s"(sbase), "s"(ldst) : "memory");
}
#define GLD(name, T, ins) DI void name(T& d, const void* sbase, unsigned voff) { asm volatile(ins : "=v"(d) : "v"(voff), "s"(sbase) : "memory"); }
GLD(gld_u16, unsigned, "global_load_ushort %0, %1, %2")
GLD(gld_u16_sc1, unsigned, "global_load_ushort %0, %1, %2 sc1")
GLD(gld_b32_sc1, unsigned, "global_load_dword %0, %1, %2 sc1")
GLD(gld_b64_sc1, u32x2, "global_load_dwordx2 %0, %1, %2 sc1")
GLD(gld_b128, u32x4, "global_load_dwordx4 %0, %1, %2")
GLD(gld_b128_sc1, u32x4, "global_load_dwordx4 %0, %1, %2 sc1")
DI void gst_b128_sc1(void* sbase, unsigned voff, u32x4 v) {
  asm volatile("global_store_dwordx4 %0, %1, %2 sc1\n\ts_nop 1" ::"v"(voff), "v"(v), "s"(sbase) : "memory");
}
DI bf16x8 ldsr128(LAS char* p) { return *(LAS bf16x8*)p; }
DI s16x4 ldsr64(LAS char* p) { return *(volatile LAS s16x4*)p; }
DI s16x4 ldstr1(LAS char* p) { return __builtin_amdgcn_ds_read_tr16_b64_v4i16((LAS s16x4*)p); }
DI bf16x8 cat(s16x4 a, s16x4 b) { return __builtin_shufflevector(a, b, 0, 1, 2, 3, 4, 5, 6, 7); }
DI bf16x8 ldstr(LAS char* p0, LAS char* p1) {
  s16x4 a = __builtin_amdgcn_ds_read_tr16_b64_v4i16((LAS s16x4*)p0);
  s16x4 b = __builtin_amdgcn_ds_read_tr16_b64_v4i16((LAS s16x4*)p1);
  return __builtin_shufflevector(a, b, 0, 1, 2, 3, 4, 5, 6, 7);
}
template <int CTRL> DI float dppf(float v) { return __builtin_bit_cast(float, __builtin_amdgcn_update_dpp(0, __builtin_bit_cast(int, v), CTRL, 0xf, 0xf, true)); }
DI float red8(float v) {
  v += dppf<0xB1>(v);
  v += dppf<0x4E>(v);
  v += dppf<0x141>(v);
  return v;
}

struct Params {
  const u16 *x, *Are, *Aim, *Bre, *Bim, *Cre, *Cim, *logdt, *dskip, *wg, *wu, *wd;
  u16* out;
  u16* hfrag;
  u16* xnT;
  u16* yT;
  u64* stats;
  float* cs;
  u64* ctl;
};
constexpr int F_P1 = 16, F_H = 128, F_Y = 384;
constexpr unsigned SPIN_CAP = 1u << 15;

DI void wait_flags(const u64* ctl, int idx, u64 tag) {
#pragma unroll 1
  for (unsigned s = 0; s < SPIN_CAP; ++s) {
    const u64 v = __hip_atomic_load(ctl + idx, RLX_AGENT);
    if (wall(v == tag)) return;
    __builtin_amdgcn_s_sleep(1);
  }
}
DI bool tag_ok(u32x2 v, u64 tag) { return wall(v[0] == (unsigned)tag && v[1] == (unsigned)(tag >> 32)); }

struct KTab { u16 v[2048]; };
constexpr KTab make_ktab() {
  KTab t{};
  for (int i = 0; i < 2048; ++i) {
    const int l = (i & ~7) + 7 - (i & 7);
    int sh = 0;
    while ((l >> sh) >= 256) ++sh;
    int q = l >> sh;
    if (sh) { const int rem = l & ((1 << sh) - 1), half = 1 << (sh - 1); if (rem > half || (rem == half && (q & 1))) ++q; }
    const int v = q << sh;
    int a = 0, b = 0;
    if (v < 256) { a = v & 7; b = v >> 3; }
    else if (v < 512) { a = 8 + ((v & 7) >> 1); b = (v & 255) >> 3; }
    else if (v < 1024) { a = 12 + ((v >> 2) & 1) + 2 * ((v >> 8) & 1); b = (v & 255) >> 3; }
    else if (v < 2048) { a = 16 + ((v >> 8) & 3); b = (v & 255) >> 3; }
    else { a = 20; b = 0; }
    t.v[i] = (u16)((a * 32 + b) * 4);
  }
  return t;
}
__device__ const KTab g_ktab = make_ktab();

struct cf { float r, i; };
DI cf cmul(cf a, cf b) { return {a.r * b.r - a.i * b.i, a.r * b.i + a.i * b.r}; }
DI cf cexp_(float zr2, float zrev, float l) {
  const float m = __builtin_amdgcn_exp2f(zr2 * l), f = __builtin_amdgcn_fractf(zrev * l);
  return {m * __builtin_amdgcn_cosf(f), m * __builtin_amdgcn_sinf(f)};
}

constexpr int P_STRIDE = 928;
DI void spare_block(const Params& p, int sp, LAS char* lds) {
  const int tid = threadIdx.x, lane = tid & 63, w = __builtin_amdgcn_readfirstlane(tid >> 6);
  const int t0 = sp * 64;
  const int row = 8 * w + (lane >> 3), pp = lane & 7;
  u32x4 v[7], wv[14]; u32x2 ep;
  gld_b64_sc1(ep, p.ctl, 0u);
#pragma unroll
  for (int i = 0; i < 7; ++i) gld_b128(v[i], p.x + i * 64, (unsigned)((t0 + row) * (D * 2) + pp * 16));
  const int js = sp < 28 ? sp : 0;
  const u16* wsrc = ((js & 1) ? p.wu : p.wg) + (js >> 1) * 128;
  WAITV(0);
  PIN(ep);
#pragma unroll
  for (int i = 0; i < 7; ++i) PIN(v[i]);
  const u64 tag = (((u64)__builtin_amdgcn_readfirstlane(ep[1]) << 32) | (unsigned)__builtin_amdgcn_readfirstlane(ep[0])) + 1;
  float s0 = 0.f, s1 = 0.f;
#pragma unroll
  for (int i = 0; i < 7; ++i) {
    s0 = dot2(v[i][0], 0x3f803f80u, s0); s1 = dot2(v[i][1], 0x3f803f80u, s1); s0 = dot2(v[i][2], 0x3f803f80u, s0); s1 = dot2(v[i][3], 0x3f803f80u, s1);
  }
  const float mean = red8(s0 + s1) * (1.f / D);
  const float mb = rbf(mean);
  float q0 = 0.f, q1 = 0.f;
#pragma unroll
  for (int i = 0; i < 7; ++i) {
    gld_b128(wv[2 * i], wsrc + (size_t)(2 * i) * (32 * DF), (unsigned)((tid >> 4) * (DF * 2) + (tid & 15) * 16));
#pragma unroll
    for (int k = 0; k < 4; ++k) v[i][k] = pk2(blo(v[i][k]) - mb, bhi(v[i][k]) - mb);
    q0 = dot2(v[i][0], v[i][0], q0); q1 = dot2(v[i][1], v[i][1], q1); q0 = dot2(v[i][2], v[i][2], q0); q1 = dot2(v[i][3], v[i][3], q1);
    SB();
  }
  const float dm = mean - mb;
  const float vb = rbf(__builtin_fmaxf(red8(q0 + q1) * (1.f / D) - dm * dm, 0.f));
  const float sd = rbf(__builtin_sqrtf(rbf(vb + 1e-5f)));
  const float rs = 1.f / sd;
  if (pp == 0) __hip_atomic_store(p.stats + t0 + row, (u64)f2u(mb) | ((u64)f2u(sd) << 32), RLX_AGENT);
#pragma unroll
  for (int i = 0; i < 7; ++i) {
    gld_b128(wv[2 * i + 1], wsrc + (size_t)(2 * i + 1) * (32 * DF), (unsigned)((tid >> 4) * (DF * 2) + (tid & 15) * 16));
    u32x4 o;
#pragma unroll
    for (int k = 0; k < 4; ++k) o[k] = pk2(blo(v[i][k]) * rs, bhi(v[i][k]) * rs);
    *(LAS u32x4*)(lds + row * P_STRIDE + (8 * i + pp) * 16) = o;
    SB();
  }
  BAR();
  const int g16 = lane >> 4, q4 = (lane & 15) >> 2, p4 = lane & 3;
  bf16x8 r[7];
  LAS char* ra = lds + (32 * (p4 >> 1) + 8 * g16 + q4) * P_STRIDE + w * 112 + (p4 & 1) * 8;
#pragma unroll
  for (int i = 0; i < 7; ++i) r[i] = ldstr(ra + i * 16, ra + i * 16 + 4 * P_STRIDE);
  const unsigned so = (unsigned)((sp * D + w * 56 + (lane & 7)) * 128 + 64 * ((lane >> 3) & 1) + 16 * g16);
#pragma unroll
  for (int i = 0; i < 7; ++i) gst_b128_sc1(p.xnT + i * 512, so, __builtin_bit_cast(u32x4, r[i]));
  LAS char* cb = lds + 64 * P_STRIDE;
  {
    const int i16 = lane & 15;
    u32x4 sel;
#pragma unroll
    for (int k = 0; k < 4; ++k) sel[k] = i16 == 2 * k ? 0x3f80u : i16 == 2 * k + 1 ? 0x3f800000u : 0u;
    f32x4 c = {0, 0, 0, 0};
    WAITV(7);
#pragma unroll
    for (int i = 0; i < 14; ++i) { PIN(wv[i]); c = MFMA16(__builtin_bit_cast(bf16x8, sel), __builtin_bit_cast(bf16x8, wv[i]), c); }
    if (lane < 32) *(LAS f32x4*)(cb + w * 512 + i16 * 32 + (lane >> 4) * 16) = c;
  }
  BAR();
  if (tid < 128 && sp < 28) {
    float c = 0.f;
#pragma unroll
    for (int r = 0; r < 8; ++r) c += *(LAS float*)(cb + r * 512 + tid * 4);
    __hip_atomic_store((unsigned*)p.cs + (js & 1) * DF + (js >> 1) * 128 + tid, f2u(c), RLX_AGENT);
  }
  WAITV(0);
  BAR();
  if (tid == 0) __hip_atomic_store(p.ctl + F_P1 + sp, tag, RLX_AGENT);
  if (sp == 0 && w == 0) {
    wait_flags(p.ctl, F_P1 + (lane & 31), tag);
#pragma unroll 1
    for (int i = 0; i < 4; ++i) { const int e = lane + 64 * i; wait_flags(p.ctl, F_H + ((e & 15) < 14 ? e : (e & ~15)), tag); }
    if (lane == 0) __hip_atomic_store(p.ctl, tag, RLX_AGENT);
  }
}

constexpr int S0 = 0, S1 = 49280, S2 = 98944;
constexpr int K_SCR = 99072, K_SW = 4096, K_TAB = K_SCR + 8 * K_SW;
constexpr int C_K = S2 + 49280, C_KCH = 7680;
constexpr int C_X = S1, C_XC = 4160, C_XCH = 4 * C_XC;
constexpr int ST = C_X + 2 * C_XCH;
DI int slot_base(int s) { return s * S1 + (s == 2 ? S2 - 2 * S1 : 0); }
constexpr int LDS_BYTES = 163840;
static_assert(C_K + 2 * C_KCH <= LDS_BYTES && ST + 16384 <= S2 && K_TAB + 2 * 672 * 4 <= C_K, "lds map");

DI void main_block(const Params& p, int xcd, int o, LAS char* lds) {
  const int tid = threadIdx.x, lane = tid & 63, w = __builtin_amdgcn_readfirstlane(tid >> 6);
  const int h = lane >> 5, l31 = lane & 31, blk = (lane >> 4) & 1, q4 = (lane & 15) >> 2, p4 = lane & 3;
  const int sg = ((p4 & 1) << 1) | (p4 >> 1);
  const int tt = 4 * (xcd >> 1) + o / 7, fs = 7 * (xcd & 1) + o % 7;
  const int cb = xcd * 28 + o;
  const int dtt = 4 * xcd + o / 7, ddt = o % 7;
  const int cl = w >> 2, wq = w & 3, ch = 2 * cb + cl, tc = tid & 255;
  const int tg = w >> 2, cg = w & 3;
  const int t0 = tt * 128;
  unsigned pf0;
  unsigned pAr, pAi, pBr, pBi, pCr, pCi, pDt, pDk; u32x4 kidx; u32x2 ep;
  {
    const unsigned no = (unsigned)(ch * NS + tc) * 2;
    gld_b64_sc1(ep, p.ctl, 0u);
    gld_u16(pDt, p.logdt, (unsigned)ch * 2); gld_u16(pAr, p.Are, no); gld_u16(pAi, p.Aim, no); gld_u16(pBr, p.Bre, no); gld_u16(pBi, p.Bim, no);
    gld_u16(pCr, p.Cre, no); gld_u16(pCi, p.Cim, no); gld_u16(pDk, p.dskip, (unsigned)ch * 2);
    gld_b128(kidx, &g_ktab, (unsigned)tc * 16);
  }
  __builtin_amdgcn_s_barrier();
  unsigned xo[2], wo[2];
#pragma unroll
  for (int i = 0; i < 2; ++i) {
    const int r = 8 * (2 * w + i) + (lane >> 3), c = (lane & 7) ^ ((r >> 1) & 7);
    xo[i] = (unsigned)((t0 + r) * (D * 2) + c * 16);
    const int kr = 4 * (2 * w + i) + (lane >> 4), cp = lane & 15;
    const int cc = (cp & 8) | ((((cp >> 1) & 3) ^ (kr & 3)) << 1) | (cp & 1);
    wo[i] = (unsigned)(kr * (DF * 2) + fs * 256 + cc * 16);
  }
  const unsigned lb = ldsa(lds) + w * 2048;
  auto stage_x = [&](int kc, int slot) {
    const unsigned b = lb + slot_base(slot);
    const char* xs = (const char*)p.x + kc * 128;
    glds16(xs, xo[0], b); glds16(xs, xo[1], b + 1024);
  };
  auto stage_g = [&](int kc, int slot) {
    const unsigned b = lb + slot_base(slot) + 16384;
    const char* gs = (const char*)p.wg + (size_t)kc * (64 * DF * 2);
    glds16(gs, wo[0], b); glds16(gs, wo[1], b + 1024);
  };
  auto stage_u = [&](int kc, int slot) {
    const unsigned b = lb + slot_base(slot) + 32768 + 128;
    const char* us = (const char*)p.wu + (size_t)kc * (64 * DF * 2);
    glds16(us, wo[0], b); glds16(us, wo[1], b + 1024);
  };
  stage_x(0, 0);
  if (tc < 160) *(LAS u32x4*)(lds + C_K + cl * C_KCH + tc * 16) = (u32x4){0, 0, 0, 0};
  WAITV(2);
  PIN(ep); PIN(pDt); PIN(pAr); PIN(pAi); PIN(pBr); PIN(pBi); PIN(pCr); PIN(pCi); PIN(pDk); PIN(kidx);
  const u64 tag = (((u64)__builtin_amdgcn_readfirstlane(ep[1]) << 32) | (unsigned)__builtin_amdgcn_readfirstlane(ep[0])) + 1;
  {
    const float dt = rbf(__builtin_amdgcn_exp2f(bf2f(pDt) * 1.4426950408889634f));
    const float zr2 = dt * bf2f(pAr) * 1.4426950408889634f, zrev_ = dt * bf2f(pAi) * 0.15915494309189535f;
    const cf cc_ = cmul({bf2f(pCr), bf2f(pCi)}, {dt * bf2f(pBr), dt * bf2f(pBi)});
    LAS char* ew = lds + K_SCR + w * K_SW;
    const unsigned ewr = ldsa(ew) + (((lane * 64) ^ ((lane & 4) << 5)) | (((lane >> 1) & 3) << 4));
    const int kq = lane >> 4;
    LAS char* erd[2];
#pragma unroll
    for (int t = 0; t < 2; ++t) erd[t] = ew + kq * 256 + ((t ^ (kq & 1)) << 7) + (q4 >> 1) * 64 + (((2 * (q4 & 1) + (p4 >> 1)) ^ ((2 * kq + t) & 3)) << 4) + (p4 & 1) * 8;
    auto put = [&](const cf* e, int n) {
      u32x4 r0 = {pk2(e[0].r, e[1].r), pk2(e[2].r, e[3].r), pk2(e[4].r, e[5].r), pk2(e[6].r, e[7].r)};
      u32x4 i0 = {pk2(e[0].i, e[1].i), pk2(e[2].i, e[3].i), pk2(e[4].i, e[5].i), pk2(e[6].i, e[7].i)};
      *(LAS u32x4*)(size_t)(ewr) = r0; *(LAS u32x4*)(size_t)(ewr ^ 32) = i0;
      if (n > 8) {
        u32x4 r1 = {pk2(e[8].r, e[9].r), pk2(e[10].r, e[11].r), pk2(e[12].r, e[13].r), pk2(e[14].r, e[15].r)};
        u32x4 i1 = {pk2(e[8].i, e[9].i), pk2(e[10].i, e[11].i), pk2(e[12].i, e[13].i), pk2(e[14].i, e[15].i)};
        *(LAS u32x4*)(size_t)(ewr ^ 16) = r1; *(LAS u32x4*)(size_t)(ewr ^ 48) = i1;
      }
      asm volatile("" ::: "memory");
    };
    auto get = [&](int s) { return ldstr(erd[0] + s * 1024, erd[1] + s * 1024); };
    bf16x8 bfr[2][4];
    {
      cf e[16], f[16];
      const float zrev = zrev_;
      e[0] = {1.f, 0.f};
      e[1] = cexp_(zr2, zrev, 8.f);
      e[4] = cexp_(zr2, zrev, 32.f);
      f[0] = cexp_(zr2, zrev, 128.f);
      stage_g(0, 0);
      e[2] = cmul(e[1], e[1]); e[8] = cmul(e[4], e[4]); e[3] = cmul(e[2], e[1]);
#pragma unroll
      for (int b = 5; b < 8; ++b) e[b] = cmul(e[4], e[b - 4]);
#pragma unroll
      for (int b = 9; b < 16; ++b) e[b] = cmul(e[8], e[b - 8]);
      stage_u(0, 0);
      put(e, 16);
#pragma unroll
      for (int s = 0; s < 4; ++s) bfr[0][s] = get(s);
      asm volatile("" ::: "memory");
#pragma unroll
      for (int b = 1; b < 16; ++b) f[b] = cmul(f[0], e[b]);
      stage_x(1, 1);
      put(f, 16);
#pragma unroll
      for (int s = 0; s < 4; ++s) bfr[1][s] = get(s);
      asm volatile("" ::: "memory");
    }
    f32x4 kt[2][2] = {};
    {
      cf e[16];
      const float zrev = -zrev_;
      const cf cc = {cc_.r, -cc_.i};
      const cf p1 = cexp_(zr2, zrev, 1.f), q1 = cexp_(zr2, zrev, 256.f), q2 = cexp_(zr2, zrev, 512.f), q4c = cexp_(zr2, zrev, 1024.f);
      const cf p2 = cmul(p1, p1), pq = cmul(p2, p2), q3 = cmul(q2, q1);
      e[0] = cc; e[1] = cmul(cc, p1); e[2] = cmul(cc, p2); e[3] = cmul(e[2], p1);
      e[4] = cmul(cc, pq); e[5] = cmul(e[4], p1); e[6] = cmul(e[4], p2); e[7] = cmul(e[6], p1);
      stage_g(1, 1);
      e[8] = cmul(e[0], q1); e[9] = cmul(e[2], q1); e[10] = cmul(e[4], q1); e[11] = cmul(e[6], q1);
      e[12] = cmul(e[0], q2); e[13] = cmul(e[4], q2); e[14] = cmul(e[0], q3); e[15] = cmul(e[4], q3);
      stage_u(1, 1);
      {
        const int q = o / 7;
        const char* src = w < 5 ? (const char*)((lane & 32) ? p.wu : p.wg) : (const char*)p.x;
        unsigned off = (unsigned)((64 * (2 + w) + 16 * q + ((lane >> 1) & 15)) * (DF * 2) + fs * 256 + (lane & 1) * 128);
        if (w >= 5) { const int id = (w - 5) * 64 + lane + (o % 7) * 92, r = id / 5 > 127 ? 127 : id / 5; off = (unsigned)((t0 + r) * (D * 2) + (2 + id % 5) * 128); }
        asm volatile("global_load_dword %0, %1, off" : "=v"(pf0) : "v"(src + off) : "memory");
      }
      put(e, 16);
      bf16x8 a0[4];
#pragma unroll
      for (int s = 0; s < 4; ++s) a0[s] = get(s);
      asm volatile("" ::: "memory");
      cf f[8];
      f[0] = cmul(e[0], q4c); f[1] = cmul(e[8], q4c); f[2] = cmul(e[12], q4c); f[3] = cmul(e[14], q4c); f[4] = cmul(f[0], q4c);
      f[5] = f[6] = f[7] = {0.f, 0.f};
      put(f, 8);
#pragma unroll
      for (int s = 0; s < 4; ++s) {
        const bf16x8 a1 = get(s);
        kt[0][0] = MFMA16(a0[s], bfr[0][s], kt[0][0]); kt[0][1] = MFMA16(a0[s], bfr[1][s], kt[0][1]);
        kt[1][0] = MFMA16(a1, bfr[0][s], kt[1][0]); kt[1][1] = MFMA16(a1, bfr[1][s], kt[1][1]);
      }
      asm volatile("" ::: "memory");
    }
    LAS char* pw = ew + (4 * (lane >> 4)) * 128 + (lane & 15) * 4;
#pragma unroll
    for (int r = 0; r < 4; ++r) { *(LAS float*)(pw + r * 128) = kt[0][0][r]; *(LAS float*)(pw + r * 128 + 64) = kt[0][1][r]; }
    if (lane < 32) {
#pragma unroll
      for (int r = 0; r < 4; ++r) { *(LAS float*)(pw + 2048 + r * 128) = kt[1][0][r]; *(LAS float*)(pw + 2048 + r * 128 + 64) = kt[1][1][r]; }
    }
    BAR();
    LAS char* tab = lds + K_TAB + cl * (672 * 4);
    LAS char* pb = lds + K_SCR + cl * 4 * K_SW + tc * 4;
#pragma unroll
    for (int i = 0; i < 3; ++i) {
      if (i < 2 || tc < 160) {
        float s = *(LAS float*)(pb + i * 1024) + *(LAS float*)(pb + K_SW + i * 1024);
        s += *(LAS float*)(pb + 2 * K_SW + i * 1024) + *(LAS float*)(pb + 3 * K_SW + i * 1024);
        *(LAS float*)(tab + tc * 4 + i * 1024) = s;
      }
    }
    BAR();
    u32x4 ko;
#pragma unroll
    for (int k = 0; k < 4; ++k) ko[k] = pk2(*(LAS float*)(tab + (kidx[k] & 0xffffu)), *(LAS float*)(tab + (kidx[k] >> 16)));
    *(LAS u32x4*)(lds + C_K + cl * C_KCH + (32 + (tc >> 2)) * 80 + (tc & 3) * 16) = ko;
  }
  int aoff[2], boff[4];
#pragma unroll
  for (int i = 0; i < 2; ++i) {
    const int a = 2 * cg + i;
    aoff[i] = 16384 + blk * (16384 + 128) + (8 * h + q4) * 256 + ((a >> 2) << 7) + (((a & 3) ^ q4) << 5) + sg * 8;
  }
  const int brow = 64 * tg + l31;
#pragma unroll
  for (int s = 0; s < 4; ++s) boff[s] = brow * 128 + (((2 * s + h) ^ ((brow >> 1) & 7)) << 4);
  f32x16 acc[2][2] = {};
  PIN(acc[0][0]); PIN(acc[0][1]); PIN(acc[1][0]); PIN(acc[1][1]);
  struct Frag { s16x4 a0l, a0h, a1l, a1h; bf16x8 b0, b1; };
  auto ldfrag = [&](Frag& f, LAS char* sb, int s) {
    f.b0 = ldsr128(sb + boff[s]); f.a0l = ldstr1(sb + aoff[0] + s * 4096); f.a0h = ldstr1(sb + aoff[0] + s * 4096 + 1024);
    f.b1 = ldsr128(sb + boff[s] + 4096); f.a1l = ldstr1(sb + aoff[1] + s * 4096); f.a1h = ldstr1(sb + aoff[1] + s * 4096 + 1024);
  };
  auto kstep = [&](const Frag& c, Frag& n, LAS char* sb, int s, bool early, auto&& g0, auto&& g1) {
    acc[0][0] = MFMA32(cat(c.a0l, c.a0h), c.b0, acc[0][0]); SB();
    n.b0 = ldsr128(sb + boff[s]); n.a0l = ldstr1(sb + aoff[0] + s * 4096); if (early) n.a0h = ldstr1(sb + aoff[0] + s * 4096 + 1024); SB();
    acc[1][0] = MFMA32(cat(c.a0l, c.a0h), c.b1, acc[1][0]); SB();
    if (!early) n.a0h = ldstr1(sb + aoff[0] + s * 4096 + 1024);
    n.b1 = ldsr128(sb + boff[s] + 4096);
    if (early) { n.a1l = ldstr1(sb + aoff[1] + s * 4096); n.a1h = ldstr1(sb + aoff[1] + s * 4096 + 1024); }
    SB();
    acc[0][1] = MFMA32(cat(c.a1l, c.a1h), c.b0, acc[0][1]); SB();
    if (!early) { n.a1l = ldstr1(sb + aoff[1] + s * 4096); n.a1h = ldstr1(sb + aoff[1] + s * 4096 + 1024); }
    g0(); SB();
    acc[1][1] = MFMA32(cat(c.a1l, c.a1h), c.b1, acc[1][1]); SB();
    g1(); SB();
  };
  auto nop = [&]() {};
  auto mm = [&](const Frag& c) {
    acc[0][0] = MFMA32(cat(c.a0l, c.a0h), c.b0, acc[0][0]); acc[1][0] = MFMA32(cat(c.a0l, c.a0h), c.b1, acc[1][0]);
    acc[0][1] = MFMA32(cat(c.a1l, c.a1h), c.b0, acc[0][1]); acc[1][1] = MFMA32(cat(c.a1l, c.a1h), c.b1, acc[1][1]);
  };
  const char* const xsrc = (const char*)p.x; const char* const gsrc = (const char*)p.wg; const char* const usrc = (const char*)p.wu;
  WAITV(7);
  BAR();
  Frag f0, f1;
  ldfrag(f0, lds, 0);
  stage_x(2, 2);
  int slot = 0;
#pragma unroll 1
  for (int c = 0; c < 4; ++c) {
    LAS char* sb = lds + slot_base(slot);
    const int nslot = slot == 2 ? 0 : slot + 1, pslot = slot == 0 ? 2 : slot - 1;
    const unsigned pb_ = lb + slot_base(pslot), cb_ = lb + slot_base(slot);
    const size_t wk = (size_t)(c + 2) * (64 * DF * 2);
    kstep(f0, f1, sb, 1, false, [&]() { glds16(gsrc + wk, wo[0], pb_ + 16384); }, [&]() { glds16(gsrc + wk, wo[1], pb_ + 17408); });
    kstep(f1, f0, sb, 2, false, [&]() { glds16(usrc + wk, wo[0], pb_ + 32896); }, [&]() { glds16(usrc + wk, wo[1], pb_ + 33920); });
    kstep(f0, f1, sb, 3, true, nop, nop);
    if (c == 0) WAITV(7); else WAITV(6);
    BAR();
    kstep(f1, f0, lds + slot_base(nslot), 0, false, [&]() { glds16(xsrc + (c + 3) * 128, xo[0], cb_); }, [&]() { glds16(xsrc + (c + 3) * 128, xo[1], cb_ + 1024); });
    slot = nslot;
  }
  PIN(pf0);
  const unsigned wdo = (unsigned)((224 * w + (lane >> 3)) * (D * 2) + ddt * 128 + (((lane & 7) ^ (((lane >> 4) & 1) << 2)) << 4));
  const unsigned wdT = ldsa(lds) + ST + w * 2048, wd6 = ldsa(lds) + w * 6144;
  const int wl = (8 * h + q4) * 128 + (((q4 >> 1) & 1) << 6) + blk * 32 + sg * 8;
  bf16x8 afr[28];
  auto wd_ld = [&](int bt, int i) {
    glds16((const char*)p.wd + (size_t)(14 * bt + i) * (8 * D * 2), wdo, i < 2 ? wdT + i * 1024 : i < 8 ? wd6 + S2 + (i - 2) * 1024 : wd6 + (i - 8) * 1024);
  };
  auto wd_rd = [&](int bt, int i) {
    LAS char* a = lds + (i < 2 ? ST + w * 2048 : w * 6144 + (i < 8 ? S2 + ((i - 2) >> 1) * 2048 : ((i - 8) >> 1) * 2048)) + (wl ^ ((i & 1) << 6));
    afr[14 * bt + i] = ldstr(a, a + 512);
  };
  const int pidx = F_P1 + l31;
  u32x2 fl;
  {
    const size_t wk = (size_t)6 * (64 * DF * 2);
    kstep(f0, f1, lds + S1, 1, false, [&]() { glds16(gsrc + wk, wo[0], lb + 16384); }, [&]() { glds16(gsrc + wk, wo[1], lb + 17408); });
    kstep(f1, f0, lds + S1, 2, false, [&]() { glds16(usrc + wk, wo[0], lb + 32896); }, [&]() { glds16(usrc + wk, wo[1], lb + 33920); });
    kstep(f0, f1, lds + S1, 3, true, nop, nop);
  }
  WAITV(6);
  BAR();
  kstep(f1, f0, lds + S2, 0, false, [&]() { wd_ld(0, 0); }, [&]() { wd_ld(0, 1); });
  kstep(f0, f1, lds + S2, 1, false, nop, [&]() { gld_b64_sc1(fl, p.ctl, (unsigned)pidx * 8); });
  kstep(f1, f0, lds + S2, 2, false, nop, nop);
  kstep(f0, f1, lds + S2, 3, true, nop, nop);
  WAITV(0); PIN(fl);
  BAR();
  if (!tag_ok(fl, tag)) wait_flags(p.ctl, pidx, tag);
  u32x2 st[2]; unsigned csv[2]; u32x4 xa, xb;
  {
    const int ia = (tc + 252) & 255, ib = (ia + 1) & 255;
    gld_b128_sc1(xa, p.xnT, (unsigned)(((ia >> 3) * D + ch) * 128 + (ia & 7) * 16)); gld_b128_sc1(xb, p.xnT, (unsigned)(((ib >> 3) * D + ch) * 128 + (ib & 7) * 16));
    gld_b64_sc1(st[0], p.stats, (unsigned)(t0 + brow) * 8); gld_b64_sc1(st[1], p.stats, (unsigned)(t0 + brow + 32) * 8);
    const int r16 = lane & 15, fo = 128 * fs + 32 * cg + 4 * (((r16 >> 2 & 1) << 1) | (r16 >> 3)) + (r16 & 3);
    gld_b32_sc1(csv[0], p.cs, (unsigned)(blk * DF + fo) * 4); gld_b32_sc1(csv[1], p.cs, (unsigned)(blk * DF + fo + 16) * 4);
  }
  SB();
  kstep(f1, f0, lds, 0, false, [&]() { wd_ld(0, 2); }, [&]() { wd_ld(0, 3); });
  kstep(f0, f1, lds, 1, false, [&]() { wd_ld(0, 4); }, [&]() { wd_ld(0, 5); });
  kstep(f1, f0, lds, 2, false, [&]() { wd_ld(0, 6); }, [&]() { wd_ld(0, 7); });
  WAITV(6);
  PIN(st[0]); PIN(st[1]); PIN(csv[0]); PIN(csv[1]); PIN(xa); PIN(xb);
  LAS char* const xw = lds + C_X + cl * C_XCH;
  unsigned xA[4] = {xa[0], xa[1], xa[2], xa[3]}, xB[2] = {tc == 3 ? 0u : xb[0], tc == 3 ? 0u : xb[1]};
  PIN(xA[1]); PIN(xA[2]); PIN(xA[3]); PIN(xB[0]); PIN(xB[1]);
  auto copy_wr = [&](int s) {
    LAS char* d = xw + s * C_XC + (tc >= 4 ? tc : 256 + tc) * 16;
    const unsigned o1 = __builtin_amdgcn_alignbit(xA[2], xA[1], 16), o2 = __builtin_amdgcn_alignbit(xA[3], xA[2], 16), o3 = __builtin_amdgcn_alignbit(xB[0], xA[3], 16);
    if (s == 0) *(LAS u32x4*)d = (u32x4){__builtin_amdgcn_alignbit(xA[1], xA[0], 16), o1, o2, o3};
    if (s == 1) *(LAS u32x4*)d = (u32x4){xA[1], xA[2], xA[3], xB[0]};
    if (s == 2) *(LAS u32x4*)d = (u32x4){o1, o2, o3, __builtin_amdgcn_alignbit(xB[1], xB[0], 16)};
    if (s == 3) *(LAS u32x4*)d = (u32x4){xA[2], xA[3], xB[0], xB[1]};
  };
  auto copy_head = [&]() {
    if (tc < 4) {
      unsigned b0 = tc == 3 ? xb[0] : 0u, b1 = tc == 3 ? xb[1] : 0u;
      PIN(b0); PIN(b1);
      LAS char* d = xw + tc * 16;
      *(LAS u32x4*)(d) = (u32x4){0u, 0u, 0u, b0 << 16};
      *(LAS u32x4*)(d + C_XC) = (u32x4){0u, 0u, 0u, b0};
      *(LAS u32x4*)(d + 2 * C_XC) = (u32x4){0u, 0u, b0 << 16, __builtin_amdgcn_alignbit(b1, b0, 16)};
      *(LAS u32x4*)(d + 3 * C_XC) = (u32x4){0u, 0u, b0, b1};
    }
  };
  kstep(f0, f1, lds, 3, true, [&]() { copy_wr(0); copy_wr(1); }, [&]() { copy_wr(2); copy_wr(3); copy_head(); });
  BAR();
  mm(f1); SB();
  wd_ld(0, 8); wd_ld(0, 9); wd_ld(0, 10); SB();
  {
    bf16x8 ca[2];
#pragma unroll
    for (int i = 0; i < 2; ++i) {
      const float c = h ? 0.f : u2f(csv[i]);
      const float c0 = rbf(c), r1 = c - c0, c1 = rbf(r1), c2 = r1 - c1;
      u32x4 t = {pk2(c0, c1), pk2(c2, 0.f), 0u, 0u};
      ca[i] = __builtin_bit_cast(bf16x8, t);
    }
#pragma unroll
    for (int j = 0; j < 2; ++j) {
      const unsigned nm = (st[j][0] >> 16) ^ 0x8000u;
      u32x4 t = {nm | (nm << 16), nm, 0u, 0u};
      bf16x8 mb = __builtin_bit_cast(bf16x8, t);
      acc[j][0] = MFMA32(ca[0], mb, acc[j][0]);
      acc[j][1] = MFMA32(ca[1], mb, acc[j][1]);
    }
  }
  SB();
  wd_ld(0, 11); wd_ld(0, 12); wd_ld(0, 13); SB();
  f32x16 y0 = {}, y1 = {};
  auto gelu = [&](auto PH) {
    constexpr int ph = decltype(PH)::value;
    const float K1 = -2.f * 0.7978845608028654f * 1.4426950408889634f, K2 = K1 * 0.044715f;
#pragma unroll
    for (int j = 0; j < 2; ++j) {
      const float sd = u2f(st[j][1]), r = __builtin_amdgcn_rcpf(sd), a1 = K1 * r, a2 = K2 * r * r * r, s2 = sd * sd;
#pragma unroll
      for (int i = 0; i < 2; ++i) {
        u32x4 ov;
#pragma unroll
        for (int k = 0; k < 2; ++k) {
          const int hk = 4 * j + 2 * i + k;
          if (ph == 1 && hk < 7) { wd_ld(1, 2 * hk); wd_ld(1, 2 * hk + 1); SB(); }
          float hv[4];
#pragma unroll
          for (int q = 0; q < 4; ++q) {
            const float gg = acc[j][i][4 * k + q], u = acc[j][i][4 * k + q + 8];
            const float ex = __builtin_amdgcn_exp2f(gg * __builtin_fmaf(gg * gg, a2, a1));
            hv[q] = (gg * u) * __builtin_amdgcn_rcpf(__builtin_fmaf(ex, s2, s2));
          }
          ov[2 * k] = pk2(hv[0], hv[1]); ov[2 * k + 1] = pk2(hv[2], hv[3]);
          SB();
        }
        const int tile = 4 * tt + 2 * tg + j, fstep = 8 * fs + 2 * cg + i;
        gst_b128_sc1(p.hfrag, (unsigned)(((tile * 112 + fstep) * 64 + lane) * 16), ov);
      }
    }
  };
  auto conv = [&](auto PH) {
    constexpr int ph = decltype(PH)::value;
    LAS char* ap = lds + C_X + cl * C_XCH + (l31 & 3) * C_XC + 48 + 8 * (l31 >> 2) - 16 * h + 64 * (8 * wq) - 32;
    LAS char* bp = lds + C_K + cl * C_KCH + (32 + l31 - 8 * wq) * 80 + 16 * h - 560;
    struct CF { s16x4 a0l, a0h, a1l, a1h; bf16x8 b0, b1; };
    CF c[2];
    __builtin_amdgcn_s_setprio(2);
    c[0].a0l = ldsr64(ap + 32); c[0].a0h = ldsr64(ap + 40); c[0].b0 = ldsr128(bp + 560); c[0].a1l = ldsr64(ap + 2080); c[0].a1h = ldsr64(ap + 2088); c[0].b1 = ldsr128(bp + 3120);
    if (ph == 1) WAITV(4);
#pragma unroll
    for (int n = 0; n < 16; ++n) {
      const CF& cc = c[n & 1]; CF& nx = c[(n + 1) & 1];
      const int u1 = (n + 1) >> 1, s1 = (n + 1) & 1, ao = 32 + 64 * u1 - 32 * s1, bo = 560 - 80 * u1 + 32 * s1, r0 = ph ? n : n - 8;
      y0 = MFMA32(cat(cc.a0l, cc.a0h), cc.b0, y0); SB();
      if (n < 15) { nx.a0l = ldsr64(ap + ao); nx.a0h = ldsr64(ap + ao + 8); }
      if (ph == 0 && n == 8) WAITV(0);
      if (r0 >= 0 && r0 < 7) wd_rd(0, 2 * r0);
      SB();
      y1 = MFMA32(cat(cc.a1l, cc.a1h), cc.b0, y1); SB();
      if (n < 15) { nx.b0 = ldsr128(bp + bo); nx.a1l = ldsr64(ap + ao + 2048); }
      if (r0 >= 0 && r0 < 7) wd_rd(0, 2 * r0 + 1);
      SB();
      y1 = MFMA32(cat(cc.a0l, cc.a0h), cc.b1, y1); SB();
      if (n < 15) { nx.a1h = ldsr64(ap + ao + 2056); nx.b1 = ldsr128(bp + bo + 2560); }
      if (ph == 1 && n >= 2) wd_ld(1, n - 2);
      SB();
    }
    __builtin_amdgcn_s_setprio(0);
  };
  using I0 = std::integral_constant<int, 0>; using I1 = std::integral_constant<int, 1>;
  if (cl == 0) { gelu(I0{}); conv(I1{}); }
  else { conv(I0{}); gelu(I1{}); }
  const int ycol = 16 * (wq & 1) + (lane & 15), yt = 32 * (32 * (wq >> 1) + ycol) + 8 * (lane >> 4);
  LAS char* xsk = lds + C_X + cl * C_XCH + 3 * C_XC + (yt + 28) * 2;
  u32x2 xs0 = *(LAS u32x2*)xsk, xs1 = *(LAS u32x2*)(xsk + 8);
  WAITV(0);
#pragma unroll
  for (int i = 0; i < 14; ++i) wd_rd(1, i);
  BAR();
  if (tid == 0) __hip_atomic_store(p.ctl + F_H + tt * 16 + fs, tag, RLX_AGENT);
  LAS char* rb = lds + cl * 32768;
#pragma unroll
  for (int q = 0; q < 4; ++q) {
    *(LAS f32x4*)(rb + (wq * 8 + q) * 1024 + lane * 16) = (f32x4){y0[4 * q], y0[4 * q + 1], y0[4 * q + 2], y0[4 * q + 3]};
    *(LAS f32x4*)(rb + (wq * 8 + 4 + q) * 1024 + lane * 16) = (f32x4){y1[4 * q], y1[4 * q + 1], y1[4 * q + 2], y1[4 * q + 3]};
  }
  BAR();
  {
    LAS char* rr = rb + ((wq >> 1) * 4 + (lane >> 4)) * 1024 + ycol * 16;
    f32x4 lo = *(LAS f32x4*)(rr), hi = *(LAS f32x4*)(rr + 512);
#pragma unroll
    for (int q = 1; q < 4; ++q) { lo += *(LAS f32x4*)(rr + q * 8192); hi += *(LAS f32x4*)(rr + q * 8192 + 512); }
    const float dk = bf2f(pDk);
    const float yv[8] = {lo[0], lo[1], lo[2], lo[3], hi[0], hi[1], hi[2], hi[3]};
    const unsigned xv[4] = {xs0[0], xs0[1], xs1[0], xs1[1]};
    u32x4 ov;
#pragma unroll
    for (int k = 0; k < 4; ++k) {
      const unsigned yb = pk2(yv[2 * k], yv[2 * k + 1]), sk = pk2(blo(xv[k]) * dk, bhi(xv[k]) * dk);
      ov[k] = pk2(blo(yb) + blo(sk), bhi(yb) + bhi(sk));
    }
    gst_b128_sc1(p.yT, (unsigned)((ch * L + yt) * 2), ov);
  }
  WAITV(0);
  if (w == 0) wait_flags(p.ctl, F_H + (dtt >> 1) * 16 + (lane < 14 ? lane : 0), tag);
  BAR();
  if (tid == 64) __hip_atomic_store(p.ctl + F_Y + cb, tag, RLX_AGENT);
  const char* hs = (const char*)p.hfrag + ((size_t)(2 * dtt) * 112 + 14 * w) * 1024;
  u32x4 hb[6][2];
  auto h_ld = [&](int ks) { gld_b128_sc1(hb[ks % 6][0], hs + ks * 1024, lane * 16); gld_b128_sc1(hb[ks % 6][1], hs + (112 + ks) * 1024, lane * 16); };
#pragma unroll
  for (int ks = 0; ks < 6; ++ks) h_ld(ks);
  const int et = 64 * dtt + 32 * ((w >> 1) & 1) + l31, ed = 64 * ddt + 32 * (w >> 2) + 16 * (w & 1) + 8 * h;
  f32x16 dacc[4] = {};
  u32x4 xv; u32x2 fy; unsigned yv[8];
  const int yidx = F_Y + 32 * ddt + l31;
  sfor<0, 14>([&](auto K_) {
    constexpr int ks = decltype(K_)::value;
    constexpr int NW[14] = {10, 10, 10, 10, 10, 10, 10, 10, 10, 9, 7, 6, 4, 2};
    waitv<NW[ks]>();
    PIN(hb[ks % 6][0]); PIN(hb[ks % 6][1]);
    const bf16x8 b0 = __builtin_bit_cast(bf16x8, hb[ks % 6][0]), b1 = __builtin_bit_cast(bf16x8, hb[ks % 6][1]);
    dacc[0] = MFMA32(afr[2 * ks], b0, dacc[0]); dacc[1] = MFMA32(afr[2 * ks], b1, dacc[1]);
    dacc[2] = MFMA32(afr[2 * ks + 1], b0, dacc[2]); dacc[3] = MFMA32(afr[2 * ks + 1], b1, dacc[3]);
    SB();
    if constexpr (ks + 6 < 14) h_ld(ks + 6);
    if constexpr (ks == 8) gld_b128(xv, p.x, (unsigned)(et * D + ed) * 2);
    if constexpr (ks == 10) gld_b64_sc1(fy, p.ctl, (unsigned)yidx * 8);
    SB();
  });
  WAITV(0); PIN(fy); PIN(xv);
  if (!tag_ok(fy, tag)) wait_flags(p.ctl, yidx, tag);
#pragma unroll
  for (int e = 0; e < 8; ++e) gld_u16_sc1(yv[e], p.yT + (size_t)e * L, (unsigned)(ed * L + et) * 2);
  {
    f32x4 lo, hi;
#pragma unroll
    for (int d = 0; d < 8; ++d) {
      const f32x16& a = dacc[d >> 1];
      const int r0 = 8 * (d & 1);
      const f32x4 v0 = {a[r0], a[r0 + 1], a[r0 + 2], a[r0 + 3]}, v1 = {a[r0 + 4], a[r0 + 5], a[r0 + 6], a[r0 + 7]};
      if (d == w) { lo = v0; hi = v1; }
      else {
        const int sl = w - (w > d ? 1 : 0);
        *(LAS f32x4*)(lds + ((d * 7 + sl) * 2 + 0) * 1024 + lane * 16) = v0;
        *(LAS f32x4*)(lds + ((d * 7 + sl) * 2 + 1) * 1024 + lane * 16) = v1;
      }
    }
    BAR();
#pragma unroll
    for (int s = 0; s < 7; ++s) {
      lo += *(LAS f32x4*)(lds + ((w * 7 + s) * 2 + 0) * 1024 + lane * 16);
      hi += *(LAS f32x4*)(lds + ((w * 7 + s) * 2 + 1) * 1024 + lane * 16);
    }
    WAITV(0);
#pragma unroll
    for (int e = 0; e < 8; ++e) PIN(yv[e]);
    const float gl[8] = {lo[0], lo[1], lo[2], lo[3], hi[0], hi[1], hi[2], hi[3]};
    u32x4 ov;
#pragma unroll
    for (int k = 0; k < 4; ++k) {
      const unsigned a = pk2(blo(xv[k]) + bf2f(yv[2 * k]), bhi(xv[k]) + bf2f(yv[2 * k + 1]));
      const unsigned b = pk2(gl[2 * k], gl[2 * k + 1]);
      ov[k] = pk2(blo(a) + blo(b), bhi(a) + bhi(b));
    }
    *(u32x4*)(p.out + (size_t)et * D + ed) = ov;
  }
}

extern __shared__ __attribute__((aligned(16))) char smem[];
__global__ __launch_bounds__(512, 2) void k_fused(Params p) {
  const int xcd = blockIdx.x & 7, o = blockIdx.x >> 3;
  {
    const int w_ = __builtin_amdgcn_readfirstlane(threadIdx.x >> 6);
    const unsigned pdst = ldsa((LAS char*)smem) + C_K + (w_ >> 2) * C_KCH + 2560 + (w_ & 3) * 1024;
    const char* pc; asm volatile("s_getpc_b64 %0" : "=s"(pc));
    glds16((const char*)((size_t)pc & ~(size_t)255), (unsigned)(((o * 8 + w_) % 27) * 1024 + (threadIdx.x & 63) * 16), pdst);
  }
  if (__builtin_expect(o < 28, 1)) main_block(p, xcd, o, (LAS char*)smem);
  else spare_block(p, xcd * 4 + (o - 28), (LAS char*)smem);
}

#if defined(__HIP_DEVICE_COMPILE__)
#pragma clang attribute pop
#endif

extern "C" void kernel_launch(void* const* d_in, const int* in_sizes, int n_in, void* d_out, int out_size, void* d_ws, size_t ws_size, hipStream_t stream) {
  Params p;
  p.x = (const u16*)d_in[0]; p.Are = (const u16*)d_in[1]; p.Aim = (const u16*)d_in[2]; p.Bre = (const u16*)d_in[3]; p.Bim = (const u16*)d_in[4];
  p.Cre = (const u16*)d_in[5]; p.Cim = (const u16*)d_in[6]; p.logdt = (const u16*)d_in[7]; p.dskip = (const u16*)d_in[8];
  p.wg = (const u16*)d_in[10]; p.wu = (const u16*)d_in[11]; p.wd = (const u16*)d_in[12];
  p.out = (u16*)d_out;
  char* ws = (char*)d_ws;
  p.ctl = (u64*)ws; p.stats = (u64*)(ws + (64 << 10)); p.cs = (float*)(ws + (128 << 10));
  p.hfrag = (u16*)(ws + (1 << 20)); p.xnT = (u16*)(ws + (9 << 20)); p.yT = (u16*)(ws + (11 << 20));
  (void)hipFuncSetAttribute((const void*)k_fused, hipFuncAttributeMaxDynamicSharedMemorySize, LDS_BYTES);
  k_fused<<<256, 512, LDS_BYTES, stream>>>(p);
}
```
